```python
import torch

def _softmax(x, axis=-1):
    return torch.softmax(x, dim=axis)
def _gelu(x):
    return torch.nn.functional.gelu(x, approximate="tanh")
def _silu(x):
    return torch.nn.functional.silu(x)
def _sigmoid(x):
    return torch.sigmoid(x)
def _layernorm(x, axis=-1, eps=1e-5):
    m = x.mean(dim=axis, keepdim=True)
    v = x.var(dim=axis, keepdim=True, unbiased=False)
    return (x - m) / torch.sqrt(v + eps)
def _scatter_add(tgt, idx, src, axis=0):
    return tgt.clone().index_add_(axis, idx, src)
def _segment_sum(src, seg, n):
    out = torch.zeros((n,) + src.shape[1:], dtype=src.dtype)
    return out.index_add_(0, seg, src)
def _pad(x, pw):
    flat = tuple(v for pair in reversed(pw) for v in pair)
    return torch.nn.functional.pad(x, flat)

kernel_name = 'sequential_preLN__sliding_block_cached__glu_gated_glu__decode'

def setup_inputs():

    _w = (lambda a: (lambda t: t.to(torch.bfloat16) if t.is_floating_point() else t)(torch.as_tensor(a)))
    def _A_setup():
        B, Sq, D = 8, 1, 1024
        Hq, Hkv, Sk, Dh = 32, 4, 4096, 128
        assert Hq % Hkv == 0, (Hq, Hkv)
        return {
            "x": _w((lambda *s: torch.randn(*s, dtype=torch.bfloat16) * 0.05)(B, Sq, D)),
            "k_cache": _w((lambda *s: torch.randn(*s, dtype=torch.bfloat16) * 3.2)(B, Hkv, Sk, Dh)),
            "v_cache": _w((lambda *s: torch.randn(*s, dtype=torch.bfloat16) * 0.4)(B, Hkv, Sk, Dh)),
            "wq": _w((lambda *s: torch.randn(*s, dtype=torch.bfloat16) * 3.2)(D, Hq * Dh) * (D ** -0.5)),
            "wo": _w((lambda *s: torch.randn(*s, dtype=torch.bfloat16) * 0.4)(Hq * Dh, D) * ((Hq * Dh) ** -0.5)),
        }
    _A_in = _A_setup()

    def _B_setup():
        B, S, D, F = 8, 1, 1024, 3584
        return {
            "x": _w((lambda *s: torch.randn(*s, dtype=torch.bfloat16) * 0.05)(B, S, D)),
            "w_gate": _w((lambda *s: torch.randn(*s, dtype=torch.bfloat16) * 0.05)(D, F)),
            "w_up": _w((lambda *s: torch.randn(*s, dtype=torch.bfloat16) * 0.05)(D, F)),
            "w_down": _w((lambda *s: torch.randn(*s, dtype=torch.bfloat16) * 0.05)(F, D)),
        }
    _B_in = _B_setup()

    return {
        "A_x": _A_in["x"],
        "A_k_cache": _A_in["k_cache"],
        "A_v_cache": _A_in["v_cache"],
        "A_wq": _A_in["wq"],
        "A_wo": _A_in["wo"],
        "B_x": _B_in["x"],
        "B_w_gate": _B_in["w_gate"],
        "B_w_up": _B_in["w_up"],
        "B_w_down": _B_in["w_down"],
    }

def reference(A_x, A_k_cache, A_v_cache, A_wq, A_wo, B_x, B_w_gate, B_w_up, B_w_down):
    def _A_ref(x, k_cache, v_cache, wq, wo):
        B, Sq, D = x.shape
        Hq, Dh = 32, 128
        Hkv, Sk = k_cache.shape[1], k_cache.shape[2]
        W = min(4096, max(256, Sk // 4))
        rep = Hq // Hkv
        q = (lambda x, *d: x.permute(*d))((x @ wq).reshape(B, Sq, Hq, Dh), 0, 2, 1, 3)
        k = (lambda x, n, axis: torch.repeat_interleave(x, n, dim=axis))(k_cache[:, :, Sk - W:, :], rep, axis=1)
        v = (lambda x, n, axis: torch.repeat_interleave(x, n, dim=axis))(v_cache[:, :, Sk - W:, :], rep, axis=1)
        scores = torch.einsum("bhqd,bhkd->bhqk", q, k) * (Dh ** -0.5)
        p = _softmax(scores, axis=-1)
        o = torch.einsum("bhqk,bhkd->bhqd", p, v)
        return (lambda x, *d: x.permute(*d))(o, 0, 2, 1, 3).reshape(B, Sq, Hq * Dh) @ wo

    def _B_ref(x, w_gate, w_up, w_down):
        g = torch.einsum("bsd,df->bsf", x, w_gate)
        u = torch.einsum("bsd,df->bsf", x, w_up)
        h = _sigmoid(g) * u
        return torch.einsum("bsf,fd->bsd", h, w_down)

    x = A_x
    h = x + _A_ref(_layernorm(x, axis=-1), A_k_cache, A_v_cache, A_wq, A_wo)
    h2 = h + _B_ref(_layernorm(h, axis=-1), B_w_gate, B_w_up, B_w_down)
    return h2
```

```cpp
#include <hip/hip_runtime.h>
#include <hip/hip_bf16.h>
typedef unsigned short u16;
__device__ __forceinline__ float bf2f(u16 v){ return __uint_as_float(((unsigned)v)<<16); }
__device__ __forceinline__ u16 f2bf(float f){ __hip_bfloat16 b = __float2bfloat16(f); return *reinterpret_cast<u16*>(&b); }
__device__ __forceinline__ float rbf(float f){ return bf2f(f2bf(f)); }

__global__ void k_ln(const u16* x, u16* y){
  __shared__ float red[256];
  int b=blockIdx.x, t=threadIdx.x;
  float v[4]; float s=0;
  for(int i=0;i<4;i++){ v[i]=bf2f(x[b*1024+t*4+i]); s+=v[i]; }
  red[t]=s; __syncthreads();
  for(int o=128;o>0;o>>=1){ if(t<o) red[t]+=red[t+o]; __syncthreads(); }
  float mean=red[0]/1024.f; __syncthreads();
  float q=0; for(int i=0;i<4;i++){ float d=v[i]-mean; q+=d*d; }
  red[t]=q; __syncthreads();
  for(int o=128;o>0;o>>=1){ if(t<o) red[t]+=red[t+o]; __syncthreads(); }
  float var=rbf(red[0]/1024.f); float m=rbf(mean);
  float den=rbf(sqrtf(rbf(var+1e-5f)));
  for(int i=0;i<4;i++) y[b*1024+t*4+i]=f2bf(rbf(v[i]-m)/den);
}
template<int MODE>
__global__ void k_gemv(const u16* a, const u16* W, const u16* W2, const u16* res, u16* out, int K, int N){
  __shared__ float red[16][8][64]; __shared__ float red2[16][8][64];
  int tx=threadIdx.x, ty=threadIdx.y; int n0=blockIdx.x*64+tx*2;
  float acc[8][2]={}; float acc2[8][2]={};
  for(int k=ty;k<K;k+=16){
    unsigned w=*(const unsigned*)(W+(size_t)k*N+n0);
    float w0=__uint_as_float(w<<16), w1=__uint_as_float(w&0xffff0000u);
    float z0=0,z1=0;
    if(MODE==2){ unsigned z=*(const unsigned*)(W2+(size_t)k*N+n0); z0=__uint_as_float(z<<16); z1=__uint_as_float(z&0xffff0000u);}
    for(int b=0;b<8;b++){ float av=bf2f(a[b*K+k]); acc[b][0]+=av*w0; acc[b][1]+=av*w1; if(MODE==2){acc2[b][0]+=av*z0; acc2[b][1]+=av*z1;} }
  }
  for(int b=0;b<8;b++){ red[ty][b][tx*2]=acc[b][0]; red[ty][b][tx*2+1]=acc[b][1]; if(MODE==2){red2[ty][b][tx*2]=acc2[b][0]; red2[ty][b][tx*2+1]=acc2[b][1];} }
  __syncthreads();
  int t=ty*32+tx;
  int b=t>>6, c=t&63; float s=0,s2=0; for(int j=0;j<16;j++){ s+=red[j][b][c]; if(MODE==2) s2+=red2[j][b][c]; }
  int n=blockIdx.x*64+c;
  if(MODE==0) out[b*N+n]=f2bf(s);
  else if(MODE==1) out[b*N+n]=f2bf(bf2f(res[b*N+n])+rbf(s));
  else { float g=rbf(s), u=rbf(s2); float sg=rbf(1.f/(1.f+__expf(-g))); out[b*N+n]=f2bf(sg*u); }
}
__global__ void k_attn(const u16* q, const u16* kc, const u16* vc, u16* o){
  __shared__ float sc[1024]; __shared__ float qs[128]; __shared__ float red[256];
  int b=blockIdx.x>>5, h=blockIdx.x&31, kv=h>>3, t=threadIdx.x;
  if(t<128) qs[t]=bf2f(q[b*4096+h*128+t]);
  __syncthreads();
  const u16* K=kc+((size_t)(b*4+kv)*4096+3072)*128; const u16* V=vc+((size_t)(b*4+kv)*4096+3072)*128;
  float mx=-INFINITY;
  for(int j=t;j<1024;j+=256){ float s=0; for(int d=0;d<128;d++) s+=qs[d]*bf2f(K[(size_t)j*128+d]); s=s*0.08838834764831845f; sc[j]=s; mx=fmaxf(mx,s);}
  red[t]=mx; __syncthreads();
  for(int of=128;of>0;of>>=1){ if(t<of) red[t]=fmaxf(red[t],red[t+of]); __syncthreads(); }
  mx=red[0]; __syncthreads();
  float sum=0; for(int j=t;j<1024;j+=256){ float e=__expf(sc[j]-mx); sc[j]=e; sum+=e; }
  red[t]=sum; __syncthreads();
  for(int of=128;of>0;of>>=1){ if(t<of) red[t]+=red[t+of]; __syncthreads(); }
  sum=red[0]; __syncthreads();
  int d=t&127, half=t>>7; float acc=0;
  for(int j=half*512;j<half*512+512;j++) acc+=(sc[j]/sum)*bf2f(V[(size_t)j*128+d]);
  red[t]=acc; __syncthreads();
  if(t<128) o[b*4096+h*128+t]=f2bf(red[t]+red[t+128]);
}
extern "C" void kernel_launch(void* const* d_in, const int* in_sizes, int n_in, void* d_out, int out_size, void* d_ws, size_t ws_size, hipStream_t stream){
  const u16* x=(const u16*)d_in[0]; const u16* kc=(const u16*)d_in[1]; const u16* vc=(const u16*)d_in[2];
  const u16* wq=(const u16*)d_in[3]; const u16* wo=(const u16*)d_in[4];
  const u16* wg=(const u16*)d_in[6]; const u16* wu=(const u16*)d_in[7]; const u16* wd=(const u16*)d_in[8];
  u16* ws=(u16*)d_ws; u16* xn=ws; u16* q=ws+8192; u16* o=q+32768; u16* h=o+32768; u16* hn=h+8192; u16* hid=hn+8192;
  k_ln<<<8,256,0,stream>>>(x,xn);
  k_gemv<0><<<64,dim3(32,16),0,stream>>>(xn,wq,nullptr,nullptr,q,1024,4096);
  k_attn<<<256,256,0,stream>>>(q,kc,vc,o);
  k_gemv<1><<<16,dim3(32,16),0,stream>>>(o,wo,nullptr,x,h,4096,1024);
  k_ln<<<8,256,0,stream>>>(h,hn);
  k_gemv<2><<<56,dim3(32,16),0,stream>>>(hn,wg,wu,nullptr,hid,1024,3584);
  k_gemv<1><<<16,dim3(32,16),0,stream>>>(hid,wd,nullptr,h,(u16*)d_out,3584,1024);
}
```

```cpp
#include <hip/hip_runtime.h>

#define GAS __attribute__((address_space(1)))
#define LAS __attribute__((address_space(3)))
typedef unsigned short u16;
typedef unsigned long long u64;
typedef unsigned v4u __attribute__((ext_vector_type(4)));
typedef unsigned v2u __attribute__((ext_vector_type(2)));
typedef float f32x4 __attribute__((ext_vector_type(4)));
typedef short v4s __attribute__((ext_vector_type(4)));
typedef short v8s __attribute__((ext_vector_type(8)));
typedef __bf16 v8bf __attribute__((ext_vector_type(8)));
typedef __bf16 v2bf __attribute__((ext_vector_type(2)));
typedef float f32x2 __attribute__((ext_vector_type(2)));

#define AUXW 2
#define DLY_Q 8
#define DLY_O 8
#define DLY_R 8
#define DLY_D 8
#define DLY_E 8
#define WO_TAIL 2
#define GU_TAIL 4

namespace {
constexpr int NCW = 8, NLW = 4, NTHR = (NCW + NLW) * 64;
constexpr int WS_EPOCH = 0;
constexpr int WS_QP = 4096;
constexpr int WS_AP = WS_QP + 4 * 8 * 4096 * 4;
constexpr int WS_ML = WS_AP + 32 * 64 * 128 * 4;
constexpr int WS_OP = WS_ML + 32 * 64 * 16;
constexpr int WS_HB = WS_OP + 32 * 8 * 1024 * 4;
constexpr int WS_GP = WS_HB + 8 * 1024 * 4;
constexpr int WS_DP = WS_GP + 56 * 16384;
constexpr int WS_END = WS_DP + 56 * 8 * 1024 * 4;
constexpr int WS_STAMP = (WS_END + 4095) / 4096 * 4096;
constexpr int L_R0 = 0, L_R1 = 32768, L_R2 = 65536, L_R3 = 98304, L_X = 131072, L_ACT = L_X + 16384, L_ACT2 = L_ACT + 4096,
              L_MX = L_ACT2 + 2048, L_LS = L_MX + 256, L_SYNC = L_LS + 256, L_END = L_SYNC + 32;
constexpr int SC1 = 16;
constexpr unsigned SPIN_CAP = 1u << 16;
constexpr float LOG2E = 1.4426950408889634f;

#define BAR() asm volatile("s_waitcnt lgkmcnt(0)\n\ts_barrier" ::: "memory")
#define VMWAIT(n) asm volatile("s_waitcnt vmcnt(%0)" ::"n"(n) : "memory")
#define SCHED() __builtin_amdgcn_sched_barrier(0)

__device__ __forceinline__ float asf(unsigned u) { return __builtin_bit_cast(float, u); }
__device__ __forceinline__ unsigned asu(float f) { return __builtin_bit_cast(unsigned, f); }
__device__ __forceinline__ unsigned pkbf(float lo, float hi) {
  f32x2 v = {lo, hi};
  return __builtin_bit_cast(unsigned, __builtin_convertvector(v, v2bf));
}
__device__ __forceinline__ float rbf(float f) { return asf(pkbf(f, f) << 16); }
template <int CTRL> __device__ __forceinline__ float dpp(float x) {
  return asf(__builtin_amdgcn_mov_dpp(asu(x), CTRL, 0xf, 0xf, true));
}
constexpr int XOR1 = 0xB1, XOR2 = 0x4E, XOR7 = 0x141, XOR8 = 0x128;
__device__ __forceinline__ float rs32(float a, float b) {
  auto r = __builtin_amdgcn_permlane32_swap(asu(a), asu(b), false, false);
  return asf(r[0]) + asf(r[1]);
}
__device__ __forceinline__ float rs16(float a, float b) {
  auto r = __builtin_amdgcn_permlane16_swap(asu(a), asu(b), false, false);
  return asf(r[0]) + asf(r[1]);
}
__device__ __forceinline__ float mx32(float a) {
  auto r = __builtin_amdgcn_permlane32_swap(asu(a), asu(a), false, false);
  return fmaxf(asf(r[0]), asf(r[1]));
}
__device__ __forceinline__ float mx16(float a) {
  auto r = __builtin_amdgcn_permlane16_swap(asu(a), asu(a), false, false);
  return fmaxf(asf(r[0]), asf(r[1]));
}
__device__ __forceinline__ float sum64(float x) {
  x += dpp<XOR1>(x);
  x += dpp<XOR2>(x);
  x += dpp<XOR7>(x);
  x += dpp<XOR8>(x);
  x = rs16(x, x);
  return rs32(x, x);
}
__device__ __forceinline__ v4u tag4(f32x4 v, unsigned T) {
  v4u o;
  o.x = __builtin_amdgcn_perm(asu(v.x), T, 0x07060500u);
  o.y = __builtin_amdgcn_perm(asu(v.y), T, 0x07060501u);
  o.z = __builtin_amdgcn_perm(asu(v.z), T, 0x07060502u);
  o.w = __builtin_amdgcn_perm(asu(v.w), T, 0x07060503u);
  return o;
}
__device__ __forceinline__ unsigned tagdiff(v4u a, unsigned T) {
  const unsigned lo = __builtin_amdgcn_perm(a.y, a.x, 0x0c0c0400u), hi = __builtin_amdgcn_perm(a.w, a.z, 0x04000c0cu);
  return (lo | hi) ^ T;
}
__device__ __forceinline__ f32x4 untag(v4u a) {
  f32x4 o = {asf(a.x & ~0xffu), asf(a.y & ~0xffu), asf(a.z & ~0xffu), asf(a.w & ~0xffu)};
  return o;
}
__device__ __forceinline__ v4u ldws(__amdgpu_buffer_rsrc_t rs, int off) { return __builtin_amdgcn_raw_buffer_load_b128(rs, off, 0, SC1); }
__device__ __forceinline__ void stws(__amdgpu_buffer_rsrc_t rs, int off, v4u v) { __builtin_amdgcn_raw_buffer_store_b128(v, rs, off, 0, SC1); }
__device__ __forceinline__ f32x4 mfma(v8s a, v4u b, f32x4 c) {
  return __builtin_amdgcn_mfma_f32_16x16x32_bf16(__builtin_bit_cast(v8bf, a), __builtin_bit_cast(v8bf, b), c, 0, 0, 0);
}
__device__ __forceinline__ v8s trfrag(LAS unsigned char *p, int off0, int off1) {
  const v4s a = __builtin_amdgcn_ds_read_tr16_b64_v4i16((LAS v4s *)(p + off0));
  const v4s b = __builtin_amdgcn_ds_read_tr16_b64_v4i16((LAS v4s *)(p + off1));
  return __builtin_shufflevector(a, b, 0, 1, 2, 3, 4, 5, 6, 7);
}
template <int NK, int KS> __device__ __forceinline__ f32x4 tile_mm(LAS unsigned char *a, LAS unsigned char *b) {
  v8s af[NK];
  v4u bf[NK];
#pragma unroll
  for (int ks = 0; ks < NK; ++ks) {
    bf[ks] = *(LAS v4u *)(b + ks * 512);
    af[ks] = trfrag(a, ks * KS, ks * KS + KS / 2);
  }
  SCHED();
  f32x4 acc0 = {0.f, 0.f, 0.f, 0.f};
#pragma unroll
  for (int ks = 0; ks < NK; ++ks) acc0 = mfma(af[ks], bf[ks], acc0);
  return acc0;
}
template <int N> __device__ __forceinline__ void gather(__amdgpu_buffer_rsrc_t rs, const int (&off)[N], v4u (&a)[N], unsigned T) {
#pragma unroll 1
  for (unsigned spins = 0;; ++spins) {
    asm volatile("" ::: "memory");
#pragma unroll
    for (int i = 0; i < N; ++i) a[i] = ldws(rs, off[i]);
    unsigned d = 0;
#pragma unroll
    for (int i = 0; i < N; ++i) d |= tagdiff(a[i], T);
    if (__all(d == 0u) || spins > SPIN_CAP) break;
  }
}
__device__ __forceinline__ __amdgpu_buffer_rsrc_t mkrs(const void *p, int bytes) {
  return __builtin_amdgcn_make_buffer_rsrc((void *)p, 0, bytes, 0x00020000);
}
__device__ __forceinline__ void dma1(__amdgpu_buffer_rsrc_t rs, LAS unsigned char *dst, int voff, int soff) {
  __builtin_amdgcn_raw_ptr_buffer_load_lds(rs, (LAS void *)dst, 16, voff, soff, 0, AUXW);
}
template <int LO, int HI> __device__ __forceinline__ void dman(__amdgpu_buffer_rsrc_t rs, LAS unsigned char *dst, int voff, int soff0, int sstep) {
#pragma unroll
  for (int i = LO; i < HI; ++i) dma1(rs, dst + 4096 * i, voff, soff0 + i * sstep);
}
__device__ __forceinline__ void lds_set(int addr, unsigned v, int l) {
  if (l == 0) asm volatile("ds_write_b32 %0, %1" ::"v"(addr), "v"(v) : "memory");
}
__device__ __forceinline__ void arrive(int k, int l) {
  if (l == 0) asm volatile("ds_add_u32 %0, %1" ::"v"(L_SYNC + 16 + 4 * k), "v"(1u) : "memory");
}
template <int K, int STAGE> __device__ __forceinline__ void await(LAS unsigned char *L) {
  for (;;) {
    const v4u f = *(volatile LAS v4u *)(L + L_SYNC);
    unsigned m = min(min(f.x, f.y), min(f.z, f.w));
    if (K >= 0) m = (*(volatile LAS unsigned *)(L + L_SYNC + 16 + 4 * (K < 0 ? 0 : K)) >= (unsigned)NCW) ? m : 0u;
    if (__builtin_amdgcn_readfirstlane(m) >= (unsigned)STAGE) break;
  }
  asm volatile("" ::: "memory");
}
#define WIN 6
template <int LO, int HI> __device__ __forceinline__ void dmap(__amdgpu_buffer_rsrc_t rs, LAS unsigned char *dst, int voff, int soff0, int sstep) {
#pragma unroll
  for (int i = LO; i < HI; ++i) {
    VMWAIT(WIN - 1);
    dma1(rs, dst + 4096 * i, voff, soff0 + i * sstep);
  }
}
#define ST(i)
#define STOUT()
}

__global__ void __launch_bounds__(NTHR, 3)
    blk_kernel(const u16 *x, const u16 *kc, const u16 *vc, const u16 *wq, const u16 *wo, const u16 *wg, const u16 *wu, const u16 *wd, u16 *out,
        unsigned char *ws) {
  extern __shared__ __attribute__((aligned(16))) unsigned char smem_[];
  LAS unsigned char *L = (LAS unsigned char *)smem_;
  const int tid = threadIdx.x, l = tid & 63, wv = __builtin_amdgcn_readfirstlane(tid >> 6);
  const int c = blockIdx.x, xcd = c & 7, j = c >> 3;
  const int kq = j & 3;
  const int cg64 = (j >> 2) * 8 + xcd;
  const int ab = xcd, akv = j >> 3, as = j & 7;
  const int ku = 4 * xcd + (j >> 3), ocg = j & 7;
  const bool isG = j < 28;
  const int fg = xcd * 7 + (j >> 2);

  if (__builtin_expect(wv < NLW, 1)) {
    const int lw = wv;
    LAS unsigned char *dst = L + lw * 1024;
    const int fl = L_SYNC + 4 * lw;
    {
      const __amdgpu_buffer_rsrc_t rx = mkrs(x, 8 * 1024 * 2);
#pragma unroll
      for (int i = 0; i < 4; ++i)
        __builtin_amdgcn_raw_ptr_buffer_load_lds(rx, (LAS void *)(dst + L_X + 4096 * i), 16, l * 16, lw * 1024 + i * 4096, 0, 0);
    }
    const int v128 = ((l & 7) ^ (((l >> 4) & 3) << 1)) * 16;
    const int r4 = 4 * lw + (l >> 4);
    dman<0, 8>(mkrs(wq, 1024 * 4096 * 2), dst + L_R0, (8 * lw + (l >> 3)) * 8192 + v128, kq * 256 * 8192 + cg64 * 128, 32 * 8192);
    ST(1);
    BAR();
    VMWAIT(8);
    lds_set(fl, 1, l);
    ST(2);
    const int kvbase = ((ab * 4 + akv) * 4096 + 3072 + as * 128) * 256;
    const __amdgpu_buffer_rsrc_t rk = mkrs(kc, 8 * 4 * 4096 * 128 * 2), rv = mkrs(vc, 8 * 4 * 4096 * 128 * 2), ro = mkrs(wo, 4096 * 1024 * 2);
    const int vk = r4 * 256 + (((l & 15) ^ r4) * 16);
    const int v256 = ((l & 15) ^ ((r4 & 7) << 1)) * 16;
    dmap<0, WIN>(rk, dst + L_R1, vk, kvbase, 4096);
    lds_set(fl, 2, l);
    ST(3);
    dmap<WIN, 8>(rk, dst + L_R1, vk, kvbase, 4096);
    dmap<0, WIN>(rv, dst + L_R2, r4 * 256 + v256, kvbase, 4096);
    lds_set(fl, 3, l);
    ST(4);
    dmap<WIN, 8>(rv, dst + L_R2, r4 * 256 + v256, kvbase, 4096);
    const int so = ((ku >> 1) * 256 + (ku & 1) * 64) * 2048 + ocg * 256;
#pragma unroll
    for (int i = 0; i < 8; ++i) {
      VMWAIT(WIN - 1);
      dma1(ro, dst + L_R3 + 4096 * i, r4 * 2048 + v256, so + ((i & 1) * 128 + (i >> 1) * 16) * 2048);
      if (i == WIN - 1) {
        lds_set(fl, 4, l);
        ST(5);
      }
    }
    VMWAIT(WO_TAIL);
    lds_set(fl, 5, l);
    ST(6);
    VMWAIT(0);
    ST(7);
    BAR();
    if (!isG) {
      STOUT();
      return;
    }
    BAR();
    {
      const int vo = (8 * lw + (l >> 3)) * 7168 + v128, sg = kq * 256 * 7168 + fg * 128;
      dman<0, 8>(mkrs(wg, 1024 * 3584 * 2), dst + L_R0, vo, sg, 32 * 7168);
      dman<0, 8>(mkrs(wu, 1024 * 3584 * 2), dst + L_R1, vo, sg, 32 * 7168);
    }
    ST(8);
    VMWAIT(GU_TAIL);
    ST(9);
    BAR();
    VMWAIT(0);
    ST(10);
    BAR();
    BAR();
    {
      const int r2 = 2 * lw + (l >> 5);
      dman<0, 8>(mkrs(wd, 3584 * 1024 * 2), dst + L_R2, r2 * 2048 + (((l & 31) ^ (r2 << 1)) * 16), fg * 64 * 2048 + kq * 512, 8 * 2048);
    }
    ST(11);
    VMWAIT(0);
    ST(12);
    BAR();
    STOUT();
    return;
  }

  const int w = wv - NLW;
  const __amdgpu_buffer_rsrc_t rs = mkrs(ws, WS_END);
  const int g = l >> 4, col = l & 15, b8 = l & 7, q4 = (l >> 2) & 3, p4 = l & 3;
  const unsigned E = __hip_atomic_load((GAS unsigned *)(ws + WS_EPOCH), __ATOMIC_RELAXED, __HIP_MEMORY_SCOPE_AGENT);
  const int bfrag = g * 128 + b8 * 16;
  const int tst = b8 * 64 + g * 16;
  const int lst = (l & 1) * 64 + g * 16;
  const int rr = xcd * 4 + kq, rb = 2 * (rr >> 3) + ((l >> 2) & 1), rcol = (rr & 7) * 128 + 32 * (w & 3) + 16 * ((l >> 3) & 1) + 4 * p4;
  v2u xr = {0u, 0u};
  {
    if (w == 0 && l < 8) *(LAS unsigned *)(L + L_SYNC + 4 * l) = 0u;
    BAR();
    await<-1, 1>(L);
    const v4u xa = *(LAS v4u *)(L + L_X + w * 2048 + l * 16), xb = *(LAS v4u *)(L + L_X + w * 2048 + 1024 + l * 16);
    xr = *(LAS v2u *)(L + L_X + (rb * 1024 + rcol) * 2);
    float v[16];
#pragma unroll
    for (int i = 0; i < 4; ++i) {
      v[2 * i] = asf(xa[i] << 16), v[2 * i + 1] = asf(xa[i] & 0xffff0000u);
      v[8 + 2 * i] = asf(xb[i] << 16), v[8 + 2 * i + 1] = asf(xb[i] & 0xffff0000u);
    }
    float s = 0.f;
#pragma unroll
    for (int i = 0; i < 16; ++i) s += v[i];
    const float mean = sum64(s) * (1.f / 1024.f);
    float qq = 0.f;
#pragma unroll
    for (int i = 0; i < 16; ++i) qq += (v[i] - mean) * (v[i] - mean);
    const float var = rbf(sum64(qq) * (1.f / 1024.f));
    const float m = rbf(mean), den = rbf(sqrtf(rbf(var + 1e-5f)));
    float y[8];
#pragma unroll
    for (int i = 0; i < 4; ++i) {
      const unsigned xs = (kq & 2) ? xb[i] : xa[i];
      y[2 * i] = rbf(asf(xs << 16) - m) / den;
      y[2 * i + 1] = rbf(asf(xs & 0xffff0000u) - m) / den;
    }
    if ((l >> 5) == (kq & 1)) {
      const int u = l & 31;
      LAS unsigned char *a = L + L_ACT + (u >> 2) * 512 + (2 * (u & 1)) * 128 + w * 16 + ((u >> 1) & 1) * 8;
      *(LAS v2u *)a = v2u{pkbf(y[0], y[1]), pkbf(y[2], y[3])};
      *(LAS v2u *)(a + 128) = v2u{pkbf(y[4], y[5]), pkbf(y[6], y[7])};
    }
  }
  const unsigned Eu = __builtin_amdgcn_readfirstlane(E);
  const unsigned T = ((Eu & 0x7f7f7f7fu) + 0x01010101u) ^ (Eu & 0x80808080u);
  const int trow = 4 * g + q4;
  const int tr128 = trow * 128 + (p4 & 1) * 8 + (((2 * (w & 3) + (p4 >> 1)) ^ (((trow >> 1) & 3) << 1)) * 16);
  const int sw256 = (trow & 7) << 1;
  const int tr256 = trow * 256 + (p4 & 1) * 8 + (((2 * w + (p4 >> 1)) ^ sw256) * 16);
  arrive(0, l);
  ST(1);
  if (w < 4) {
    await<0, 2>(L);
    ST(2);
    const f32x4 acc = tile_mm<8, 4096>(L + L_R0 + tr128, L + L_ACT + bfrag);
    if (col < 8)
      stws(rs, WS_QP + (((((cg64 >> 4) * 4 + (b8 >> 1)) * 8 + ((cg64 >> 1) & 7)) * 4 + kq) * 8 + (cg64 & 1) * 4 + w) * 128 + lst, tag4(acc, T));
  }
  ST(3);
  await<-1, 3>(L);
  float sc[4];
  {
    int off[2];
    v4u a[2];
#pragma unroll
    for (int i = 0; i < 2; ++i) off[i] = WS_QP + ((((akv * 4 + (ab >> 1)) * 8 + w) * 4 + g) * 8 + i * 4 + q4) * 128 + (ab & 1) * 64 + p4 * 16;
    gather<2>(rs, off, a, T);
    ST(4);
    const f32x4 u0 = untag(a[0]), u1 = untag(a[1]);
    const float t0 = rs32(u0.x, u1.x), t1 = rs32(u0.y, u1.y), t2 = rs32(u0.z, u1.z), t3 = rs32(u0.w, u1.w);
    const float e0 = rs16(t0, t2), e1 = rs16(t1, t3);
    const int d = (l >> 5) * 64 + col * 4 + (g & 1) * 2;
    *(LAS unsigned *)(L + L_ACT2 + (d >> 5) * 512 + ((d >> 3) & 3) * 128 + w * 16 + (d & 7) * 2) = pkbf(e0, e1);
    arrive(1, l);
    await<1, 3>(L);
    ST(5);
    LAS unsigned char *kp = L + L_R1 + (16 * w + col) * 256;
    v8s kf[4];
    v4u qf[4];
#pragma unroll
    for (int i = 0; i < 4; ++i) {
      kf[i] = *(LAS v8s *)(kp + (((4 * i + g) ^ col) * 16));
      qf[i] = *(LAS v4u *)(L + L_ACT2 + bfrag + i * 512);
    }
    SCHED();
    f32x4 acc = {0.f, 0.f, 0.f, 0.f};
#pragma unroll
    for (int i = 0; i < 4; ++i) acc = mfma(kf[i], qf[i], acc);
#pragma unroll
    for (int r = 0; r < 4; ++r) sc[r] = rbf(rbf(acc[r]) * 0.08838834764831845f);
  }
  float M;
  {
    float m = fmaxf(fmaxf(sc[0], sc[1]), fmaxf(sc[2], sc[3]));
    m = mx32(mx16(m));
    if (l < 8) *(LAS float *)(L + L_MX + l * 32 + w * 4) = m;
    arrive(2, l);
    await<2, 3>(L);
    const f32x4 m0 = *(LAS f32x4 *)(L + L_MX + b8 * 32), m1 = *(LAS f32x4 *)(L + L_MX + b8 * 32 + 16);
    M = fmaxf(fmaxf(fmaxf(m0.x, m0.y), fmaxf(m0.z, m0.w)), fmaxf(fmaxf(m1.x, m1.y), fmaxf(m1.z, m1.w)));
    float p[4];
#pragma unroll
    for (int r = 0; r < 4; ++r) p[r] = __builtin_amdgcn_exp2f((sc[r] - M) * LOG2E);
    if (col < 8) *(LAS v2u *)(L + L_ACT + (w >> 1) * 512 + bfrag + (w & 1) * 8) = v2u{pkbf(p[0], p[1]), pkbf(p[2], p[3])};
    float s = (p[0] + p[1]) + (p[2] + p[3]);
    s = rs16(s, s);
    s = rs32(s, s);
    if (l < 8) *(LAS float *)(L + L_LS + l * 32 + w * 4) = s;
    arrive(3, l);
    await<3, 4>(L);
  }
  ST(6);
  {
    const f32x4 acc = tile_mm<4, 8192>(L + L_R2 + tr256, L + L_ACT + bfrag);
    const int slot = akv * 64 + ab * 8 + as;
    if (col < 8) stws(rs, WS_AP + (((akv * 8 + (b8 >> 1) * 2 + (w >> 2)) * 64 + ab * 8 + as) * 4 + (w & 3)) * 128 + lst, tag4(acc, T));
    if (w == 7 && l < 8) {
      const f32x4 s0 = *(LAS f32x4 *)(L + L_LS + l * 32), s1 = *(LAS f32x4 *)(L + L_LS + l * 32 + 16);
      const float s = ((s0.x + s0.y) + (s0.z + s0.w)) + ((s1.x + s1.y) + (s1.z + s1.w));
      stws(rs, WS_ML + slot * 128 + l * 16, tag4(f32x4{M, s, 0.f, 0.f}, T));
    }
  }
  ST(7);
  await<-1, 5>(L);
  {
    int off[5];
    v4u a[5];
    const int slot0 = (ku >> 3) * 64 + w * 8 + 4 * (l >> 5), hp = (ku >> 1) & 3;
#pragma unroll
    for (int i = 0; i < 4; ++i) off[i] = WS_AP + (ku * 64 + w * 8 + 4 * (l >> 5) + i) * 512 + (l & 31) * 16;
    off[4] = WS_ML + (slot0 + p4) * 128 + (2 * hp + ((l >> 2) & 1)) * 16;
    gather<5>(rs, off, a, T);
    ST(8);
    const float ms = asf(a[4].x & ~0xffu), ls = asf(a[4].y & ~0xffu);
    float mm = fmaxf(ms, dpp<XOR1>(ms));
    mm = fmaxf(mm, dpp<XOR2>(mm));
    mm = mx32(mm);
    const float wt = __builtin_amdgcn_exp2f((ms - mm) * LOG2E);
    float ll = wt * ls;
    ll += dpp<XOR1>(ll);
    ll += dpp<XOR2>(ll);
    ll = rs32(ll, ll);
    const float cf = wt * __builtin_amdgcn_rcpf(ll);
    const float c0 = dpp<0x00>(cf), c1 = dpp<0x55>(cf), c2 = dpp<0xAA>(cf), c3 = dpp<0xFF>(cf);
    const f32x4 o = untag(a[0]) * c0 + untag(a[1]) * c1 + untag(a[2]) * c2 + untag(a[3]) * c3;
    const float e0 = rs32(o.x, o.z), e1 = rs32(o.y, o.w);
    *(LAS unsigned *)(L + L_ACT2 + ((l >> 3) & 3) * 512 + p4 * 128 + w * 16 + ((l >> 2) & 1) * 8 + (l >> 5) * 4) = pkbf(e0, e1);
    BAR();
    ST(9);
    const f32x4 acc = tile_mm<4, 8192>(L + L_R3 + tr256, L + L_ACT2 + bfrag);
    if (col < 8) stws(rs, WS_OP + ((((b8 >> 1) * 32 + ocg * 4 + (w >> 1)) * 32 + ku) * 2 + (w & 1)) * 128 + lst, tag4(acc, T));
  }
  ST(10);
  if (!isG) {
    if (w < 4) {
      __builtin_amdgcn_s_sleep(DLY_R);
      int off[8];
      v4u a[8];
#pragma unroll
      for (int i = 0; i < 8; ++i) off[i] = WS_OP + (rr * 4 + w) * 8192 + i * 1024 + l * 16;
      gather<8>(rs, off, a, T);
      ST(11);
      f32x4 s = ((untag(a[0]) + untag(a[1])) + (untag(a[2]) + untag(a[3]))) + ((untag(a[4]) + untag(a[5])) + (untag(a[6]) + untag(a[7])));
#pragma unroll
      for (int i = 0; i < 4; ++i) {
        const float t = rs16(s[i], s[i]);
        s[i] = rs32(t, t);
      }
      f32x4 h;
      h.x = rbf(asf(xr.x << 16) + rbf(s.x));
      h.y = rbf(asf(xr.x & 0xffff0000u) + rbf(s.y));
      h.z = rbf(asf(xr.y << 16) + rbf(s.z));
      h.w = rbf(asf(xr.y & 0xffff0000u) + rbf(s.w));
      if (l < 16) stws(rs, WS_HB + (rb * 1024 + rcol) * 4, tag4(h, T));
      ST(12);
    }
  } else {
    const int dcu = xcd * 28 + j;
    const int eu = (w == 0) ? dcu : 224 + dcu;
    const bool hasE = (w == 0) || (w == 1 && dcu < 32);
    f32x4 hE;
    BAR();
    BAR();
    {
      int off[5];
      v4u a[5];
#pragma unroll
      for (int i = 0; i < 4; ++i) off[i] = WS_HB + (w * 1024 + 256 * ((kq + i) & 3) + 4 * l) * 4;
      off[4] = WS_HB + (((hasE ? eu : 0) * 32) + 4 * b8) * 4;
      gather<5>(rs, off, a, T);
      ST(11);
      if (c == 0 && w == 0 && l == 0) __hip_atomic_store((GAS unsigned *)(ws + WS_EPOCH), T, __ATOMIC_RELAXED, __HIP_MEMORY_SCOPE_AGENT);
      hE = untag(a[4]);
      const f32x4 h0 = untag(a[0]), h1 = untag(a[1]), h2 = untag(a[2]), h3 = untag(a[3]);
      const f32x4 sv = (h0 + h1) + (h2 + h3);
      const float mean = sum64((sv.x + sv.y) + (sv.z + sv.w)) * (1.f / 1024.f);
      const f32x4 d0 = h0 - mean, d1 = h1 - mean, d2 = h2 - mean, d3 = h3 - mean;
      const f32x4 qv = (d0 * d0 + d1 * d1) + (d2 * d2 + d3 * d3);
      const float var = rbf(sum64((qv.x + qv.y) + (qv.z + qv.w)) * (1.f / 1024.f));
      const float m = rbf(mean), ri = __builtin_amdgcn_rcpf(rbf(__builtin_amdgcn_sqrtf(rbf(var + 1e-5f))));
      *(LAS v2u *)(L + L_ACT + (l >> 3) * 512 + (l & 3) * 128 + w * 16 + ((l >> 2) & 1) * 8) =
          v2u{pkbf(rbf(h0.x - m) * ri, rbf(h0.y - m) * ri), pkbf(rbf(h0.z - m) * ri, rbf(h0.w - m) * ri)};
      BAR();
      ST(12);
      const f32x4 acc = tile_mm<8, 4096>(L + L_R0 + (w >> 2) * 32768 + tr128, L + L_ACT + bfrag);
      if (col < 8) stws(rs, WS_GP + ((fg * 4 + kq) * 8 + w) * 512 + tst, tag4(acc, T));
    }
    BAR();
    __builtin_amdgcn_s_sleep(DLY_D);
    {
      int off[2];
      v4u a[2];
#pragma unroll
      for (int i = 0; i < 2; ++i) off[i] = WS_GP + ((fg * 4 + g) * 8 + 4 * i + 2 * (w & 1) + (col >> 3)) * 512 + (w >> 1) * 128 + b8 * 16;
      gather<2>(rs, off, a, T);
      ST(13);
      const f32x4 gg = untag(a[0]), uu = untag(a[1]);
      const float g1 = rbf(rs16(rs32(gg.x, gg.z), rs32(gg.y, gg.w))), u1 = rbf(rs16(rs32(uu.x, uu.z), rs32(uu.y, uu.w)));
      const float sg = rbf(__builtin_amdgcn_rcpf(1.f + __builtin_amdgcn_exp2f(-g1 * LOG2E)));
      *(LAS u16 *)(L + L_ACT2 + (w & 1) * 512 + p4 * 128 + (2 * (w >> 1) + ((l >> 2) & 1)) * 16 + (col >> 3) * 8 + g * 2) =
          (u16)pkbf(sg * u1, 0.f);
      BAR();
      LAS unsigned char *ap = L + L_R2 + trow * 512 + (p4 & 1) * 8;
      const int c0 = ((4 * w + (p4 >> 1)) ^ sw256) * 16, c1 = ((4 * w + 2 + (p4 >> 1)) ^ sw256) * 16;
      const v4u b0 = *(LAS v4u *)(L + L_ACT2 + bfrag), b1 = *(LAS v4u *)(L + L_ACT2 + bfrag + 512);
      const v8s a00 = trfrag(ap + c0, 0, 8192), a10 = trfrag(ap + c1, 0, 8192), a01 = trfrag(ap + c0, 16384, 24576),
                a11 = trfrag(ap + c1, 16384, 24576);
      SCHED();
      f32x4 a0 = {0.f, 0.f, 0.f, 0.f}, a1 = {0.f, 0.f, 0.f, 0.f};
      a0 = mfma(a00, b0, a0);
      a1 = mfma(a10, b0, a1);
      a0 = mfma(a01, b1, a0);
      a1 = mfma(a11, b1, a1);
      stws(rs, WS_DP + ((b8 * 32 + 8 * kq + w) * 56 + fg) * 128 + (col >> 3) * 64 + g * 16, tag4(col < 8 ? a0 : a1, T));
    }
    ST(14);
    if (hasE) {
      __builtin_amdgcn_s_sleep(DLY_E);
      int off[7];
      v4u a[7];
#pragma unroll
      for (int i = 0; i < 7; ++i) off[i] = WS_DP + eu * 7168 + i * 1024 + l * 16;
      gather<7>(rs, off, a, T);
      f32x4 s = ((untag(a[0]) + untag(a[1])) + (untag(a[2]) + untag(a[3]))) + ((untag(a[4]) + untag(a[5])) + untag(a[6]));
#pragma unroll
      for (int i = 0; i < 4; ++i) {
        float t = s[i];
        t += dpp<XOR8>(t);
        t = rs16(t, t);
        s[i] = rs32(t, t);
      }
      if (l < 8) *(GAS v2u *)(out + eu * 32 + 4 * l) = v2u{pkbf(hE.x + rbf(s.x), hE.y + rbf(s.y)), pkbf(hE.z + rbf(s.z), hE.w + rbf(s.w))};
    }
  }
  STOUT();
}

extern "C" void kernel_launch(void *const *d_in, const int *in_sizes, int n_in, void *d_out, int out_size, void *d_ws, size_t ws_size,
    hipStream_t stream) {
  (void)in_sizes, (void)n_in, (void)out_size, (void)ws_size;
  (void)hipFuncSetAttribute((const void *)blk_kernel, hipFuncAttributeMaxDynamicSharedMemorySize, L_END);
  blk_kernel<<<256, NTHR, L_END, stream>>>((const u16 *)d_in[0], (const u16 *)d_in[1], (const u16 *)d_in[2], (const u16 *)d_in[3],
      (const u16 *)d_in[4], (const u16 *)d_in[6], (const u16 *)d_in[7], (const u16 *)d_in[8], (u16 *)d_out, (unsigned char *)d_ws);
}
```

```cpp
#include <hip/hip_runtime.h>

#define GAS __attribute__((address_space(1)))
#define LAS __attribute__((address_space(3)))
typedef unsigned short u16;
typedef unsigned long long u64;
typedef unsigned v4u __attribute__((ext_vector_type(4)));
typedef unsigned v2u __attribute__((ext_vector_type(2)));
typedef float f32x4 __attribute__((ext_vector_type(4)));
typedef short v4s __attribute__((ext_vector_type(4)));
typedef short v8s __attribute__((ext_vector_type(8)));
typedef __bf16 v8bf __attribute__((ext_vector_type(8)));
typedef __bf16 v2bf __attribute__((ext_vector_type(2)));
typedef float f32x2 __attribute__((ext_vector_type(2)));

#define AUXW 2
#define WIN 6
#define EPOFF 0
#define WIN2 8
#define DLY_Q 0
#define DLY_R 12
#define DLY_D 8
#define DLY_E 14
#define H_TAIL 3
#define WG_EARLY 0
#define PREW 3
#define WARM 0
#define CODE_BYTES 10496

namespace {
constexpr int NCW = 8, NLW = 4, NTHR = (NCW + NLW) * 64;
constexpr int WS_EPOCH = 0;
constexpr int WS_QP = 4096;
constexpr int WS_AP = WS_QP + 4 * 8 * 4096 * 4;
constexpr int WS_ML = WS_AP + 32 * 64 * 128 * 4;
constexpr int WS_OP = WS_ML + 32 * 64 * 16;
constexpr int WS_HB = WS_OP + 32 * 8 * 1024 * 4;
constexpr int WS_GP = WS_HB + 8 * 1024 * 4;
constexpr int WS_DP = WS_GP + 56 * 16384;
constexpr int WS_END = WS_DP + 56 * 8 * 1024 * 4;
constexpr int WS_STAMP = (WS_END + 4095) / 4096 * 4096;
constexpr int OOB = 0x40000000;
constexpr int L_R0 = 0, L_R1 = 32768, L_R2 = 65536, L_R3 = 98304, L_X = 131072, L_ACT = L_X + 16384, L_ACT2 = L_ACT + 4096,
              L_MX = L_ACT2 + 2048, L_LS = L_MX + 256, L_SYNC = L_LS + 256, L_EP = L_SYNC + 64, L_JUNK = L_EP + 256, L_END = L_JUNK + 4096;
enum { C_X = 0, C_XN, C_Q, C_K, C_QL, C_MX, C_P, C_V, C_WO, C_H, C_N };
constexpr int SC1 = 16;
constexpr unsigned SPIN_CAP = 1u << 16;
constexpr float LOG2E = 1.4426950408889634f;

#define BAR() asm volatile("s_waitcnt lgkmcnt(0)\n\ts_barrier" ::: "memory")
#define VMWAIT(n) asm volatile("s_waitcnt vmcnt(%0)" ::"n"(n) : "memory")
#define SCHED() __builtin_amdgcn_sched_barrier(0)
#define PIN(x) asm volatile("" : "+v"(x))

__device__ __forceinline__ float asf(unsigned u) { return __builtin_bit_cast(float, u); }
__device__ __forceinline__ unsigned asu(float f) { return __builtin_bit_cast(unsigned, f); }
__device__ __forceinline__ unsigned pkbf(float lo, float hi) {
  f32x2 v = {lo, hi};
  return __builtin_bit_cast(unsigned, __builtin_convertvector(v, v2bf));
}
__device__ __forceinline__ float blo(unsigned p) { return asf(p << 16); }
__device__ __forceinline__ float bhi(unsigned p) { return asf(p & 0xffff0000u); }
__device__ __forceinline__ float rbf(float f) { return blo(pkbf(f, f)); }
__device__ __forceinline__ float fmx(float a, float b) { return __builtin_fmaxf(a, b); }
template <int CTRL> __device__ __forceinline__ float dpp(float x) {
  return asf(__builtin_amdgcn_mov_dpp(asu(x), CTRL, 0xf, 0xf, true));
}
constexpr int XOR1 = 0xB1, XOR2 = 0x4E, XOR7 = 0x141, XOR8 = 0x128;
__device__ __forceinline__ float rs32(float a, float b) {
  auto r = __builtin_amdgcn_permlane32_swap(asu(a), asu(b), false, false);
  return asf(r[0]) + asf(r[1]);
}
__device__ __forceinline__ float rs16(float a, float b) {
  auto r = __builtin_amdgcn_permlane16_swap(asu(a), asu(b), false, false);
  return asf(r[0]) + asf(r[1]);
}
__device__ __forceinline__ float mx32(float a) {
  auto r = __builtin_amdgcn_permlane32_swap(asu(a), asu(a), false, false);
  return fmx(asf(r[0]), asf(r[1]));
}
__device__ __forceinline__ float mx16(float a) {
  auto r = __builtin_amdgcn_permlane16_swap(asu(a), asu(a), false, false);
  return fmx(asf(r[0]), asf(r[1]));
}
__device__ __forceinline__ float sum64(float x) {
  x += dpp<XOR1>(x);
  x += dpp<XOR2>(x);
  x += dpp<XOR7>(x);
  x += dpp<XOR8>(x);
  x = rs16(x, x);
  return rs32(x, x);
}
struct Tag {
  unsigned t0, t1, t2, t3;
  unsigned m;
};
__device__ __forceinline__ v4u tag4(f32x4 v, const Tag &T) {
  v4u o;
  o.x = (T.m & T.t0) | (~T.m & asu(v.x));
  o.y = (T.m & T.t1) | (~T.m & asu(v.y));
  o.z = (T.m & T.t2) | (~T.m & asu(v.z));
  o.w = (T.m & T.t3) | (~T.m & asu(v.w));
  return o;
}
__device__ __forceinline__ v4u ldws(__amdgpu_buffer_rsrc_t rs, int off) { return __builtin_amdgcn_raw_buffer_load_b128(rs, off, 0, SC1); }
__device__ __forceinline__ void stws(__amdgpu_buffer_rsrc_t rs, int off, v4u v) { __builtin_amdgcn_raw_buffer_store_b128(v, rs, off, 0, SC1); }
__device__ __forceinline__ f32x4 mfma(v8s a, v4u b, f32x4 c) {
  return __builtin_amdgcn_mfma_f32_16x16x32_bf16(__builtin_bit_cast(v8bf, a), __builtin_bit_cast(v8bf, b), c, 0, 0, 0);
}
__device__ __forceinline__ v8s trfrag(LAS unsigned char *p, int off0, int off1) {
  const v4s a = __builtin_amdgcn_ds_read_tr16_b64_v4i16((LAS v4s *)(p + off0));
  const v4s b = __builtin_amdgcn_ds_read_tr16_b64_v4i16((LAS v4s *)(p + off1));
  return __builtin_shufflevector(a, b, 0, 1, 2, 3, 4, 5, 6, 7);
}
template <int NK, int KS> __device__ __forceinline__ void rd_a(LAS unsigned char *a, v8s (&af)[NK]) {
#pragma unroll
  for (int ks = 0; ks < NK; ++ks) af[ks] = trfrag(a, ks * KS, ks * KS + KS / 2);
}
template <int NK> __device__ __forceinline__ void rd_b(LAS unsigned char *b, v4u (&bf)[NK]) {
#pragma unroll
  for (int ks = 0; ks < NK; ++ks) bf[ks] = *(LAS v4u *)(b + ks * 512);
}
template <int NK> __device__ __forceinline__ f32x4 mm(const v8s (&af)[NK], const v4u (&bf)[NK]) {
  SCHED();
  f32x4 acc = {0.f, 0.f, 0.f, 0.f};
#pragma unroll
  for (int ks = 0; ks < NK; ++ks) acc = mfma(af[ks], bf[ks], acc);
  return acc;
}
template <int NK> __device__ __forceinline__ void pin_a(v8s (&af)[NK]) {
#pragma unroll
  for (int ks = 0; ks < NK; ++ks) PIN(af[ks]);
}
template <int N, int STEP, unsigned MASK = 0xffu, bool ZW = false>
__device__ __forceinline__ void gather(__amdgpu_buffer_rsrc_t rs, int off, f32x4 (&v)[N], const Tag &T) {
  unsigned spins = 0;
#pragma unroll 1
  for (;;) {
    asm volatile("" ::: "memory");
    v4u a[N];
#pragma unroll
    for (int i = 0; i < N; ++i) a[i] = ldws(rs, off + i * STEP);
    unsigned d = 0;
#pragma unroll
    for (int i = 0; i < N; ++i) {
      a[i].x ^= T.t0, a[i].y ^= T.t1, a[i].z ^= T.t2, a[i].w ^= T.t3;
      d |= (a[i].x | a[i].y) | (a[i].z | a[i].w);
      v[i] = __builtin_bit_cast(f32x4, a[i]);
    }
    d &= MASK;
    if (ZW) d |= a[0].z | a[0].w;
    if (__builtin_expect(__builtin_amdgcn_ballot_w64(d != 0u) == 0ull, 1)) break;
    if (++spins > SPIN_CAP) break;
  }
}
__device__ __forceinline__ __amdgpu_buffer_rsrc_t mkrs(const void *p, int bytes) {
  return __builtin_amdgcn_make_buffer_rsrc((void *)p, 0, bytes, 0x00020000);
}
template <int AUX, int IMM = 0> __device__ __forceinline__ void dma1(__amdgpu_buffer_rsrc_t rs, LAS unsigned char *dst, int voff, int soff) {
  __builtin_amdgcn_raw_ptr_buffer_load_lds(rs, (LAS void *)dst, 16, voff, soff, IMM, AUX);
}
template <int W> __device__ __forceinline__ void dmal(__amdgpu_buffer_rsrc_t rs, LAS unsigned char *&dst, int voff, int &soff, int sstep, int n) {
#pragma unroll 1
  for (int i = 0; i < n; ++i) {
    if (W < 64) VMWAIT(W - 1);
    dma1<AUXW>(rs, dst, voff, soff);
    dst += 1024, soff += sstep;
  }
}
template <int K> __device__ __forceinline__ void arrive(int sa, unsigned one = 1u) {
  asm volatile("ds_add_u32 %0, %1 offset:%2" ::"v"(sa), "v"(one), "n"(4 * K) : "memory");
}
template <int K, int N, int SLEEP = 0> __device__ __forceinline__ void await(LAS unsigned char *syn) {
  while (__builtin_amdgcn_readfirstlane(*(volatile LAS unsigned *)(syn + 4 * K)) < (unsigned)N) {
    if (SLEEP) __builtin_amdgcn_s_sleep(SLEEP);
  }
  asm volatile("" ::: "memory");
}
#define ST(i)
#define STOUT()
}

__global__ void __launch_bounds__(NTHR, 3)
    blk_kernel(const u16 *x, const u16 *wq, const u16 *kc, const u16 *vc, const u16 *wo, const u16 *wg, const u16 *wu, const u16 *wd, u16 *out,
        unsigned char *ws) {
  extern __shared__ __attribute__((aligned(16))) unsigned char smem_[];
  LAS unsigned char *L = (LAS unsigned char *)smem_;
  u64 pc;
  asm volatile("s_getpc_b64 %0" : "=s"(pc));
  const int tid = threadIdx.x, l = tid & 63, wv = __builtin_amdgcn_readfirstlane(tid >> 6);
  const int c = blockIdx.x, xcd = c & 7, j = c >> 3;
  const int kq = j & 3;
  const int cg64 = (j >> 2) * 8 + xcd;
  const int ab = xcd, akv = j >> 3, as = j & 7;
  const int ku = 4 * xcd + (j >> 3), ocg = j & 7;
  const bool isG = j < 28;
  const int fg = xcd * 7 + (j >> 2);
  int sa = l == 0 ? L_SYNC : L_JUNK + 4 * l;

  const int lw = wv & 3;
  asm volatile("ds_write_b32 %0, %1" ::"v"(L_SYNC + 4 * (l & 15)), "v"(0u) : "memory");
  const int v128 = ((l & 7) ^ (((l >> 4) & 3) << 1)) * 16;
  int vx = l * 16, vq = (l >> 3) * 8192 + v128;
  int so = (kq * 256 + lw * 64) * 8192 + cg64 * 128, sx = lw * 4096;
  LAS unsigned char *d = L + L_R0 + lw * 8192, *dx = L + L_X + lw * 4096;
  asm volatile("" : "+v"(vx), "+v"(vq), "+s"(so), "+s"(sx), "+s"(d), "+s"(dx));
  asm volatile("s_waitcnt lgkmcnt(0)\n\ts_barrier" ::"s"(x), "s"(wq), "s"(kc), "s"(vc), "s"(wo), "s"(wg), "s"(wu), "s"(wd), "s"(out), "s"(ws)
               : "memory");
  if (__builtin_expect(wv < NLW, 1)) {
    {
      const __amdgpu_buffer_rsrc_t rx = mkrs(x, 8 * 1024 * 2);
      dma1<0, 0>(rx, dx, vx, sx);
      dma1<0, 1024>(rx, dx, vx, sx);
      dma1<0, 2048>(rx, dx, vx, sx);
      dma1<0, 3072>(rx, dx, vx, sx);
    }
    dmal<64>(mkrs(wq, 1024 * 4096 * 2), d, vq, so, 8 * 8192, 8);
    __builtin_amdgcn_raw_ptr_buffer_load_lds(mkrs(ws, 4096), (LAS void *)(L + L_EP), 4, 0, 0, 0, SC1);
    ST(1);
    VMWAIT(9);
    arrive<C_X>(sa);
    ST(2);
    const int kvbase = ((ab * 4 + akv) * 4096 + 3072 + as * 128 + lw * 32) * 256;
    const int r4 = l >> 4;
    const int v256 = ((l & 15) ^ ((4 * (lw & 1) + r4) << 1)) * 16;
    {
      const __amdgpu_buffer_rsrc_t rk = mkrs(kc, 8 * 4 * 4096 * 128 * 2);
      const int vk = r4 * 256 + (((l & 15) ^ (4 * lw + r4)) * 16);
      d += 32768 - 8192, so = kvbase;
      dmal<WIN>(rk, d, vk, so, 1024, WIN);
      arrive<C_Q>(sa);
      ST(3);
      dmal<WIN>(rk, d, vk, so, 1024, 8 - WIN);
    }
    {
      const __amdgpu_buffer_rsrc_t rv = mkrs(vc, 8 * 4 * 4096 * 128 * 2);
      d += 32768 - 8192, so = kvbase;
      dmal<WIN>(rv, d, r4 * 256 + v256, so, 1024, WIN);
      arrive<C_K>(sa);
      ST(4);
      dmal<WIN>(rv, d, r4 * 256 + v256, so, 1024, 8 - WIN);
    }
    {
      const __amdgpu_buffer_rsrc_t ro = mkrs(wo, 4096 * 1024 * 2);
      so = ((ku >> 1) * 256 + (ku & 1) * 64 + 4 * lw) * 2048 + ocg * 256;
      d += 32768 - 8192;
#pragma unroll 1
      for (int i = 0; i < 8; ++i) {
        VMWAIT(WIN - 1);
        if (i == WIN - 1) arrive<C_V>(sa);
        dma1<AUXW>(ro, d + i * 1024, r4 * 2048 + v256, so + ((i & 1) * 128 + (i >> 1) * 16) * 2048);
      }
    }
    ST(5);
    VMWAIT(0);
    arrive<C_WO>(sa);
    ST(6);
    BAR();
    ST(7);
    if (!isG) {
      STOUT();
      return;
    }
    const int sg = (kq * 256 + lw * 64) * 7168 + fg * 128;
    d = L + L_R0 + lw * 8192, so = sg;
    dmal<WIN2>(mkrs(wg, 1024 * 3584 * 2), d, (l >> 3) * 7168 + v128, so, 8 * 7168, 8);
    d += 32768 - 8192, so = sg;
    dmal<WIN2>(mkrs(wu, 1024 * 3584 * 2), d, (l >> 3) * 7168 + v128, so, 8 * 7168, 8);
    ST(8);
    {
      d += 32768 - 8192, so = (fg * 64 + 2 * lw) * 2048 + kq * 512;
      dmal<WIN2>(mkrs(wd, 3584 * 1024 * 2), d, (l >> 5) * 2048 + (((l & 31) ^ ((2 * lw + (l >> 5)) << 1)) * 16), so, 8 * 2048, 8);
    }
    ST(9);
    VMWAIT(H_TAIL);
    arrive<C_H>(sa);
    ST(10);
    VMWAIT(0);
    ST(11);
    STOUT();
    return;
  }

  const int w = wv - NLW;
  const int g = l >> 4, col = l & 15, b8 = l & 7, q4 = (l >> 2) & 3, p4 = l & 3;
  const int lst = (l & 1) * 64 + g * 16;
  const int dead = col < 8 ? 0 : OOB;
  const int trow = 4 * g + q4;
  const int sw128 = ((trow >> 1) & 3) << 1, sw256 = (trow & 7) << 1;
  LAS unsigned char *hw = L + L_ACT + (l >> 3) * 512 + (l & 3) * 128 + w * 16 + ((l >> 2) & 1) * 8;
  LAS unsigned char *bact = L + L_ACT + g * 128 + b8 * 16;
  LAS unsigned char *a128 = L + trow * 128 + (p4 & 1) * 8 + (((2 * (w & 3) + (p4 >> 1)) ^ sw128) * 16) + (w >> 2) * 32768;
  LAS unsigned char *syn = L + L_SYNC;
  int o_qs = WS_QP + (((((cg64 >> 4) * 4 + (b8 >> 1)) * 8 + ((cg64 >> 1) & 7)) * 4 + kq) * 8 + (cg64 & 1) * 4 + w) * 128 + lst + dead;
  LAS unsigned char *xa_ = L + L_X + w * 2048 + l * 16, *xo_ = L + L_X + w * 2048 + kq * 512 + l * 8;
  unsigned one = 1u;
  Tag T, T4;
  T.m = 0xffu, T4.m = 0xfu;
  const __amdgpu_buffer_rsrc_t rs = mkrs(ws, WS_END);
  asm volatile("" : "+v"(xa_), "+v"(xo_), "+v"(hw), "+v"(bact), "+v"(a128), "+v"(syn), "+v"(o_qs), "+v"(one), "+v"(T4.m), "+v"(sa));
  await<C_X, NLW>(syn);
  ST(1);
  {
    const v4u xa = *(LAS v4u *)xa_, xb = *(LAS v4u *)(xa_ + 1024);
    const v2u xo = *(LAS v2u *)xo_;
    f32x2 v[8];
#pragma unroll
    for (int i = 0; i < 4; ++i) {
      v[i] = f32x2{blo(xa[i]), bhi(xa[i])};
      v[4 + i] = f32x2{blo(xb[i]), bhi(xb[i])};
    }
    const f32x2 s = ((v[0] + v[1]) + (v[2] + v[3])) + ((v[4] + v[5]) + (v[6] + v[7]));
    const float mean = sum64(s.x + s.y) * (1.f / 1024.f);
    f32x2 qq = {0.f, 0.f};
#pragma unroll
    for (int i = 0; i < 8; ++i) qq += (v[i] - mean) * (v[i] - mean);
    const float var = rbf(sum64(qq.x + qq.y) * (1.f / 1024.f));
    const float m = rbf(mean), ri = __builtin_amdgcn_rcpf(rbf(__builtin_amdgcn_sqrtf(rbf(var + 1e-5f))));
    const unsigned d0 = pkbf(blo(xo.x) - m, bhi(xo.x) - m), d1 = pkbf(blo(xo.y) - m, bhi(xo.y) - m);
    *(LAS v2u *)hw = v2u{pkbf(blo(d0) * ri, bhi(d0) * ri), pkbf(blo(d1) * ri, bhi(d1) * ri)};
  }
  arrive<C_XN>(sa, one);
  ST(2);
  unsigned Ev;
  if (w < 4) {
    await<C_XN, NCW>(syn);
    v8s af[8];
    v4u bf[8];
    rd_b<8>(bact, bf);
    await<C_Q, NLW>(syn);
    Ev = *(LAS unsigned *)(syn + (L_EP - L_SYNC));
    rd_a<8, 4096>(a128, af);
    SCHED();
    const unsigned E0 = __builtin_amdgcn_readfirstlane(Ev);
    T4.t0 = E0 + 1u, T4.t1 = (E0 >> 8) + 1u, T4.t2 = (E0 >> 16) + 1u, T4.t3 = (E0 >> 24) + 1u;
    stws(rs, o_qs, tag4(mm<8>(af, bf), T4));
  } else {
    await<C_Q, NLW, 2>(syn);
    Ev = *(LAS unsigned *)(syn + (L_EP - L_SYNC));
  }
  ST(3);
  const unsigned E = __builtin_amdgcn_readfirstlane(Ev);
  const unsigned Tw = ((E & 0x7f7f7f7fu) + 0x01010101u) ^ (E & 0x80808080u);
  T.t0 = Tw & 0xffu, T.t1 = (Tw >> 8) & 0xffu, T.t2 = (Tw >> 16) & 0xffu, T.t3 = Tw >> 24;
  T4.t0 = T.t0 & 0xfu, T4.t1 = T.t1 & 0xfu, T4.t2 = T.t2 & 0xfu, T4.t3 = T.t3 & 0xfu;
  SCHED();
  const int rr = xcd * 4 + kq, rb = 2 * (rr >> 3) + ((l >> 2) & 1), rcol = (rr & 7) * 128 + 32 * (w & 3) + 16 * ((l >> 3) & 1) + 4 * p4;
  const int dcu = xcd * 28 + j, eu = (w == 0) ? dcu : 224 + dcu;
  const bool hasE = (w == 0) || (w == 1 && dcu < 32);
  const int drow = 16 * ((trow >> 1) & 3) + 2 * (g >> 1) + (trow & 1);
  LAS unsigned char *a256 = L + L_R2 + (32 * g + q4) * 256 + (p4 & 1) * 8 + (((2 * w + (p4 >> 1)) ^ sw256) * 16);
  LAS unsigned char *kp[4];
#pragma unroll
  for (int i = 0; i < 4; ++i) kp[i] = L + L_R1 + (32 * q4 + 4 * w + p4) * 256 + (((4 * i + g) ^ col) * 16);
  LAS unsigned char *qw = L + L_ACT2 + ((l >> 3) & 3) * 512 + ((l >> 1) & 3) * 128 + w * 16 + (l & 1) * 8 + (l >> 5) * 4;
  LAS unsigned char *mxw = L + (l < 8 ? L_MX + l * 32 + w * 4 : L_JUNK + 256 + 4 * l), *mxr = L + L_MX + b8 * 32;
  LAS unsigned char *pw = col < 8 ? bact + (w >> 1) * 512 + (w & 1) * 8 : L + L_JUNK + 1024 + 8 * l;
  LAS unsigned char *ow = L + L_ACT2 + ((l >> 3) & 3) * 512 + p4 * 128 + w * 16 + ((l >> 2) & 1) * 8 + (l >> 5) * 4;
  LAS unsigned char *dw = L + L_ACT2 + (w & 1) * 512 + p4 * 128 + (2 * (w >> 1) + ((l >> 2) & 1)) * 16 + (col >> 3) * 8 + g * 2;
  LAS unsigned char *ad0 = L + L_R2 + drow * 512 + (p4 & 1) * 8 + (((4 * w + (p4 >> 1)) ^ sw256) * 16);
  LAS unsigned char *ad1 = L + L_R2 + drow * 512 + (p4 & 1) * 8 + (((4 * w + 2 + (p4 >> 1)) ^ sw256) * 16);
  int o_qg = WS_QP + ((akv * 4 + (ab >> 1)) * 8 + w) * 4096 + (l >> 2) * 128 + (ab & 1) * 64 + p4 * 16;
  int o_as = WS_AP + (((akv * 8 + (b8 >> 1) * 2 + (w >> 2)) * 64 + ab * 8 + as) * 4 + (w & 3)) * 128 + lst + dead;
  int o_ms = WS_ML + (akv * 64 + ab * 8 + as) * 128 + l * 16 + (l < 8 ? 0 : OOB);
  int o_mg = WS_ML + ((ku >> 3) * 64 + w * 8 + 4 * (l >> 5) + p4) * 128 + (2 * ((ku >> 1) & 3) + ((l >> 2) & 1)) * 16;
  int o_ag = WS_AP + (ku * 64 + w * 8 + 4 * (l >> 5)) * 512 + (l & 31) * 16;
  int o_os = WS_OP + ((((b8 >> 1) * 32 + ocg * 4 + (w >> 1)) * 32 + ku) * 2 + (w & 1)) * 128 + lst + dead;
  int o_s2 = isG ? WS_GP + ((fg * 4 + kq) * 8 + w) * 512 + b8 * 64 + g * 16 + dead : WS_HB + (rb * 1024 + rcol) * 4 + (l < 16 && w < 4 ? 0 : OOB);
  int o_g2 = isG ? WS_HB + w * 4096 + l * 16 : WS_OP + (rr * 4 + w) * 8192 + l * 16;
  int o_ds = isG ? WS_DP + ((b8 * 32 + 8 * kq + w) * 56 + fg) * 128 + (col >> 3) * 64 + g * 16 : OOB;
  int o_gg = WS_GP + ((fg * 4 + g) * 8 + 2 * (w & 1) + (col >> 3)) * 512 + (w >> 1) * 128 + b8 * 16;
  int o_eh = WS_HB + eu * 128 + b8 * 16, o_dg = WS_DP + eu * 7168 + l * 16, o_out = (eu * 32 + 4 * b8 + g) * 2 + dead;
  v2u xr = *(LAS v2u *)(L + L_X + (rb * 1024 + rcol) * 2);
  asm volatile("" : "+v"(xr), "+v"(a256), "+v"(kp[0]), "+v"(kp[1]), "+v"(kp[2]), "+v"(kp[3]), "+v"(qw), "+v"(mxw), "+v"(mxr), "+v"(pw), "+v"(ow),
               "+v"(dw), "+v"(ad0), "+v"(ad1));
  asm volatile("" : "+v"(o_qg), "+v"(o_as), "+v"(o_ms), "+v"(o_mg), "+v"(o_ag), "+v"(o_os), "+v"(o_s2), "+v"(o_g2), "+v"(o_ds), "+v"(o_gg),
               "+v"(o_eh), "+v"(o_dg), "+v"(o_out), "+v"(T.m));
  const v4u bad = {~T.t0, ~T.t1, ~T.t2, ~T.t3};
  stws(rs, o_as, bad);
  await<C_K, NLW, 1>(syn);
  __builtin_amdgcn_s_sleep(DLY_Q);
  float M;
  v4u pf[4];
  {
    v8s kf[4];
#pragma unroll
    for (int i = 0; i < 4; ++i) kf[i] = *(LAS v8s *)kp[i];
    f32x4 a[2];
    gather<2, 2048, 0xfu>(rs, o_qg, a, T4);
    ST(4);
    const f32x4 s = a[0] + a[1];
    *(LAS unsigned *)qw = pkbf(rs32(s.x, s.z), rs32(s.y, s.w));
    arrive<C_QL>(sa, one);
    await<C_QL, NCW>(syn);
    ST(5);
    v4u qf[4];
    rd_b<4>(bact + 4096, qf);
    const f32x4 acc = mm<4>(kf, qf);
    const unsigned r0 = pkbf(acc.x, acc.y), r1 = pkbf(acc.z, acc.w);
    const float SCL = 0.08838834764831845f;
    const unsigned s0 = pkbf(blo(r0) * SCL, bhi(r0) * SCL), s1 = pkbf(blo(r1) * SCL, bhi(r1) * SCL);
    const float sc0 = blo(s0), sc1 = bhi(s0), sc2 = blo(s1), sc3 = bhi(s1);
    *(LAS float *)mxw = mx32(mx16(fmx(fmx(sc0, sc1), fmx(sc2, sc3))));
    arrive<C_MX>(sa, one);
    await<C_MX, NCW>(syn);
    const f32x4 m0 = *(LAS f32x4 *)mxr, m1 = *(LAS f32x4 *)(mxr + 16);
    M = fmx(fmx(fmx(m0.x, m0.y), fmx(m0.z, m0.w)), fmx(fmx(m1.x, m1.y), fmx(m1.z, m1.w)));
    const float p0 = __builtin_amdgcn_exp2f((sc0 - M) * LOG2E), p1 = __builtin_amdgcn_exp2f((sc1 - M) * LOG2E),
                p2 = __builtin_amdgcn_exp2f((sc2 - M) * LOG2E), p3 = __builtin_amdgcn_exp2f((sc3 - M) * LOG2E);
    *(LAS v2u *)pw = v2u{pkbf(p0, p1), pkbf(p2, p3)};
    float ls = (p0 + p1) + (p2 + p3);
    ls = rs16(ls, ls);
    *(LAS float *)(mxw + 256) = rs32(ls, ls);
    arrive<C_P>(sa, one);
    await<C_P, NCW>(syn);
    rd_b<4>(bact, pf);
    if (w == 7) {
      const f32x4 l2 = *(LAS f32x4 *)(mxr + 256) + *(LAS f32x4 *)(mxr + 272);
      stws(rs, o_ms, tag4(f32x4{M, (l2.x + l2.y) + (l2.z + l2.w), 0.f, 0.f}, T));
    }
  }
  ST(6);
  await<C_V, NLW>(syn);
  {
    v8s af[4];
    rd_a<4, 2048>(a256, af);
    stws(rs, o_as, tag4(mm<4>(af, pf), T));
  }
  ST(7);
  stws(rs, o_os, bad);
  stws(rs, o_s2, bad);
  stws(rs, o_ds, bad);
  {
    f32x4 ml[1];
    gather<1, 0, 0xffu, true>(rs, o_mg, ml, T);
    float mm_ = fmx(ml[0].x, dpp<XOR1>(ml[0].x));
    mm_ = mx32(fmx(mm_, dpp<XOR2>(mm_)));
    const float wt = __builtin_amdgcn_exp2f((ml[0].x - mm_) * LOG2E);
    float ll = wt * ml[0].y;
    ll += dpp<XOR1>(ll);
    ll += dpp<XOR2>(ll);
    ll = rs32(ll, ll);
    const float cf = wt * __builtin_amdgcn_rcpf(ll);
    float c0 = dpp<0x00>(cf), c1 = dpp<0x55>(cf), c2 = dpp<0xAA>(cf), c3 = dpp<0xFF>(cf);
    asm volatile("" : "+v"(c0), "+v"(c1), "+v"(c2), "+v"(c3));
    await<C_WO, NLW>(syn);
    ST(8);
    v8s af[4];
    rd_a<4, 2048>(a256 + 32768, af);
    f32x4 a[4];
    gather<4, 512>(rs, o_ag, a, T);
    ST(9);
    const f32x4 o = (a[0] * c0 + a[1] * c1) + (a[2] * c2 + a[3] * c3);
    *(LAS unsigned *)ow = pkbf(rs32(o.x, o.z), rs32(o.y, o.w));
    BAR();
    v4u bf[4];
    rd_b<4>(bact + 4096, bf);
    stws(rs, o_os, tag4(mm<4>(af, bf), T));
  }
  ST(10);
  if (!isG) {
    if (w < 4) {
      __builtin_amdgcn_s_sleep(DLY_R);
      f32x4 a[8];
      gather<8, 1024>(rs, o_g2, a, T);
      ST(11);
      f32x4 s = ((a[0] + a[1]) + (a[2] + a[3])) + ((a[4] + a[5]) + (a[6] + a[7]));
#pragma unroll
      for (int i = 0; i < 4; ++i) {
        const float t = rs16(s[i], s[i]);
        s[i] = rs32(t, t);
      }
      const unsigned t0 = pkbf(s.x, s.y), t1 = pkbf(s.z, s.w);
      const unsigned h0 = pkbf(blo(xr.x) + blo(t0), bhi(xr.x) + bhi(t0)), h1 = pkbf(blo(xr.y) + blo(t1), bhi(xr.y) + bhi(t1));
      stws(rs, o_s2, v4u{(h0 << 16) | T.t0, (h0 & 0xffff0000u) | T.t1, (h1 << 16) | T.t2, (h1 & 0xffff0000u) | T.t3});
      ST(12);
    }
  } else {
    await<C_H, NLW>(syn);
    {
      v8s af[8];
      rd_a<8, 4096>(a128, af);
      f32x4 h[4];
      gather<4, 1024>(rs, o_g2, h, T);
      ST(11);
      const f32x4 sv = (h[0] + h[1]) + (h[2] + h[3]);
      const float mean = sum64((sv.x + sv.y) + (sv.z + sv.w)) * (1.f / 1024.f);
      const f32x4 d0 = h[0] - mean, d1 = h[1] - mean, d2 = h[2] - mean, d3 = h[3] - mean;
      const f32x4 qv = (d0 * d0 + d1 * d1) + (d2 * d2 + d3 * d3);
      const float var = rbf(sum64((qv.x + qv.y) + (qv.z + qv.w)) * (1.f / 1024.f));
      const float m = rbf(mean), ri = __builtin_amdgcn_rcpf(rbf(__builtin_amdgcn_sqrtf(rbf(var + 1e-5f))));
      const f32x4 ho = (kq & 2) ? ((kq & 1) ? h[3] : h[2]) : ((kq & 1) ? h[1] : h[0]);
      const unsigned e0 = pkbf(ho.x - m, ho.y - m), e1 = pkbf(ho.z - m, ho.w - m);
      *(LAS v2u *)hw = v2u{pkbf(blo(e0) * ri, bhi(e0) * ri), pkbf(blo(e1) * ri, bhi(e1) * ri)};
      BAR();
      ST(12);
      v4u bf[8];
      rd_b<8>(bact, bf);
      stws(rs, o_s2, tag4(mm<8>(af, bf), T));
    }
    if (c == 0 && w == 2 && l == 0) __hip_atomic_store((GAS unsigned *)ws, Tw, __ATOMIC_RELAXED, __HIP_MEMORY_SCOPE_AGENT);
    float hv = 0.f;
    if (hasE) {
      f32x4 hE[1];
      gather<1, 0>(rs, o_eh, hE, T);
      hv = (g & 2) ? ((g & 1) ? hE[0].w : hE[0].z) : ((g & 1) ? hE[0].y : hE[0].x);
    }
    __builtin_amdgcn_s_sleep(DLY_D);
    {
      v8s a0[2], a1[2];
      rd_a<2, 4096>(ad0, a0);
      rd_a<2, 4096>(ad1, a1);
      f32x4 a[2];
      gather<2, 2048>(rs, o_gg, a, T);
      ST(13);
      const unsigned gu = pkbf(rs16(rs32(a[0].x, a[0].z), rs32(a[0].y, a[0].w)), rs16(rs32(a[1].x, a[1].z), rs32(a[1].y, a[1].w)));
      const float sg = rbf(__builtin_amdgcn_rcpf(1.f + __builtin_amdgcn_exp2f(blo(gu) * -LOG2E)));
      *(LAS u16 *)dw = (u16)pkbf(sg * bhi(gu), 0.f);
      BAR();
      v4u bf[2];
      rd_b<2>(bact + 4096, bf);
      SCHED();
      f32x4 c0 = {0.f, 0.f, 0.f, 0.f}, c1 = {0.f, 0.f, 0.f, 0.f};
      c0 = mfma(a0[0], bf[0], c0);
      c1 = mfma(a1[0], bf[0], c1);
      c0 = mfma(a0[1], bf[1], c0);
      c1 = mfma(a1[1], bf[1], c1);
      stws(rs, o_ds, tag4(col < 8 ? c0 : c1, T));
    }
    ST(14);
    if (hasE) {
      __builtin_amdgcn_s_sleep(DLY_E);
      f32x4 a[7];
      gather<7, 1024>(rs, o_dg, a, T);
      const f32x4 s = ((a[0] + a[1]) + (a[2] + a[3])) + ((a[4] + a[5]) + a[6]);
      float t = rs16(rs32(s.x, s.z), rs32(s.y, s.w));
      t += dpp<XOR8>(t);
      __builtin_amdgcn_raw_buffer_store_b16((u16)pkbf(hv + rbf(t), 0.f), mkrs(out, 8 * 1024 * 2), o_out, 0, 0);
    }
  }
  STOUT();
}

extern "C" void kernel_launch(void *const *d_in, const int *in_sizes, int n_in, void *d_out, int out_size, void *d_ws, size_t ws_size,
    hipStream_t stream) {
  (void)in_sizes, (void)n_in, (void)out_size, (void)ws_size;
  (void)hipFuncSetAttribute((const void *)blk_kernel, hipFuncAttributeMaxDynamicSharedMemorySize, L_END);
  blk_kernel<<<256, NTHR, L_END, stream>>>((const u16 *)d_in[0], (const u16 *)d_in[3], (const u16 *)d_in[1], (const u16 *)d_in[2],
      (const u16 *)d_in[4], (const u16 *)d_in[6], (const u16 *)d_in[7], (const u16 *)d_in[8], (u16 *)d_out, (unsigned char *)d_ws);
}
```

```cpp
#include <hip/hip_runtime.h>

#define GAS __attribute__((address_space(1)))
#define LAS __attribute__((address_space(3)))
typedef unsigned short u16;
typedef unsigned long long u64;
typedef unsigned v4u __attribute__((ext_vector_type(4)));
typedef unsigned v2u __attribute__((ext_vector_type(2)));
typedef float f32x4 __attribute__((ext_vector_type(4)));
typedef short v4s __attribute__((ext_vector_type(4)));
typedef short v8s __attribute__((ext_vector_type(8)));
typedef __bf16 v8bf __attribute__((ext_vector_type(8)));
typedef __bf16 v2bf __attribute__((ext_vector_type(2)));
typedef float f32x2 __attribute__((ext_vector_type(2)));

#define AUXW 2
#define WIN 6
#define EPOFF 0
#define WIN2 8
#define DLY_Q 0
#define DLY_R 12
#define DLY_D 8
#define DLY_E 14
#define H_TAIL 3
#define WG_EARLY 0
#define PREW 3
#define WARM 1
#define CODE_BYTES 10496

namespace {
constexpr int NCW = 8, NLW = 4, NTHR = (NCW + NLW) * 64;
constexpr int WS_EPOCH = 0;
constexpr int WS_QP = 4096;
constexpr int WS_AP = WS_QP + 4 * 8 * 4096 * 4;
constexpr int WS_ML = WS_AP + 32 * 64 * 128 * 4;
constexpr int WS_OP = WS_ML + 32 * 64 * 16;
constexpr int WS_HB = WS_OP + 32 * 8 * 1024 * 4;
constexpr int WS_GP = WS_HB + 8 * 1024 * 4;
constexpr int WS_DP = WS_GP + 56 * 16384;
constexpr int WS_END = WS_DP + 56 * 8 * 1024 * 4;
constexpr int WS_STAMP = (WS_END + 4095) / 4096 * 4096;
constexpr int OOB = 0x40000000;
constexpr int L_R0 = 0, L_R1 = 32768, L_R2 = 65536, L_R3 = 98304, L_X = 131072, L_ACT = L_X + 16384, L_ACT2 = L_ACT + 4096,
              L_MX = L_ACT2 + 2048, L_LS = L_MX + 256, L_SYNC = L_LS + 256, L_EP = L_SYNC + 64, L_JUNK = L_EP + 256, L_END = L_JUNK + 4096;
enum { C_X = 0, C_XN, C_Q, C_K, C_QL, C_MX, C_P, C_V, C_WO, C_H, C_N };
constexpr int SC1 = 16;
constexpr unsigned SPIN_CAP = 1u << 16;
constexpr float LOG2E = 1.4426950408889634f;

#define BAR() asm volatile("s_waitcnt lgkmcnt(0)\n\ts_barrier" ::: "memory")
#define VMWAIT(n) asm volatile("s_waitcnt vmcnt(%0)" ::"n"(n) : "memory")
#define SCHED() __builtin_amdgcn_sched_barrier(0)
#define PIN(x) asm volatile("" : "+v"(x))

__device__ __forceinline__ float asf(unsigned u) { return __builtin_bit_cast(float, u); }
__device__ __forceinline__ unsigned asu(float f) { return __builtin_bit_cast(unsigned, f); }
__device__ __forceinline__ unsigned pkbf(float lo, float hi) {
  f32x2 v = {lo, hi};
  return __builtin_bit_cast(unsigned, __builtin_convertvector(v, v2bf));
}
__device__ __forceinline__ float blo(unsigned p) { return asf(p << 16); }
__device__ __forceinline__ float bhi(unsigned p) { return asf(p & 0xffff0000u); }
__device__ __forceinline__ float rbf(float f) { return blo(pkbf(f, f)); }
__device__ __forceinline__ float fmx(float a, float b) { return __builtin_fmaxf(a, b); }
template <int CTRL> __device__ __forceinline__ float dpp(float x) {
  return asf(__builtin_amdgcn_mov_dpp(asu(x), CTRL, 0xf, 0xf, true));
}
constexpr int XOR1 = 0xB1, XOR2 = 0x4E, XOR7 = 0x141, XOR8 = 0x128;
__device__ __forceinline__ float rs32(float a, float b) {
  auto r = __builtin_amdgcn_permlane32_swap(asu(a), asu(b), false, false);
  return asf(r[0]) + asf(r[1]);
}
__device__ __forceinline__ float rs16(float a, float b) {
  auto r = __builtin_amdgcn_permlane16_swap(asu(a), asu(b), false, false);
  return asf(r[0]) + asf(r[1]);
}
__device__ __forceinline__ float mx32(float a) {
  auto r = __builtin_amdgcn_permlane32_swap(asu(a), asu(a), false, false);
  return fmx(asf(r[0]), asf(r[1]));
}
__device__ __forceinline__ float mx16(float a) {
  auto r = __builtin_amdgcn_permlane16_swap(asu(a), asu(a), false, false);
  return fmx(asf(r[0]), asf(r[1]));
}
__device__ __forceinline__ float sum64(float x) {
  x += dpp<XOR1>(x);
  x += dpp<XOR2>(x);
  x += dpp<XOR7>(x);
  x += dpp<XOR8>(x);
  x = rs16(x, x);
  return rs32(x, x);
}
struct Tag {
  unsigned t0, t1, t2, t3;
  unsigned m;
};
__device__ __forceinline__ v4u tag4(f32x4 v, const Tag &T) {
  v4u o;
  o.x = (T.m & T.t0) | (~T.m & asu(v.x));
  o.y = (T.m & T.t1) | (~T.m & asu(v.y));
  o.z = (T.m & T.t2) | (~T.m & asu(v.z));
  o.w = (T.m & T.t3) | (~T.m & asu(v.w));
  return o;
}
__device__ __forceinline__ v4u ldws(__amdgpu_buffer_rsrc_t rs, int off) { return __builtin_amdgcn_raw_buffer_load_b128(rs, off, 0, SC1); }
__device__ __forceinline__ void stws(__amdgpu_buffer_rsrc_t rs, int off, v4u v) { __builtin_amdgcn_raw_buffer_store_b128(v, rs, off, 0, SC1); }
__device__ __forceinline__ f32x4 mfma(v8s a, v4u b, f32x4 c) {
  return __builtin_amdgcn_mfma_f32_16x16x32_bf16(__builtin_bit_cast(v8bf, a), __builtin_bit_cast(v8bf, b), c, 0, 0, 0);
}
__device__ __forceinline__ v8s trfrag(LAS unsigned char *p, int off0, int off1) {
  const v4s a = __builtin_amdgcn_ds_read_tr16_b64_v4i16((LAS v4s *)(p + off0));
  const v4s b = __builtin_amdgcn_ds_read_tr16_b64_v4i16((LAS v4s *)(p + off1));
  return __builtin_shufflevector(a, b, 0, 1, 2, 3, 4, 5, 6, 7);
}
template <int NK, int KS> __device__ __forceinline__ void rd_a(LAS unsigned char *a, v8s (&af)[NK]) {
#pragma unroll
  for (int ks = 0; ks < NK; ++ks) af[ks] = trfrag(a, ks * KS, ks * KS + KS / 2);
}
template <int NK> __device__ __forceinline__ void rd_b(LAS unsigned char *b, v4u (&bf)[NK]) {
#pragma unroll
  for (int ks = 0; ks < NK; ++ks) bf[ks] = *(LAS v4u *)(b + ks * 512);
}
template <int NK> __device__ __forceinline__ f32x4 mm(const v8s (&af)[NK], const v4u (&bf)[NK]) {
  SCHED();
  f32x4 acc = {0.f, 0.f, 0.f, 0.f};
#pragma unroll
  for (int ks = 0; ks < NK; ++ks) acc = mfma(af[ks], bf[ks], acc);
  return acc;
}
template <int NK> __device__ __forceinline__ void pin_a(v8s (&af)[NK]) {
#pragma unroll
  for (int ks = 0; ks < NK; ++ks) PIN(af[ks]);
}
template <int N, int STEP, unsigned MASK = 0xffu, bool ZW = false>
__device__ __forceinline__ void gather(__amdgpu_buffer_rsrc_t rs, int off, f32x4 (&v)[N], const Tag &T) {
  unsigned spins = 0;
#pragma unroll 1
  for (;;) {
    asm volatile("" ::: "memory");
    v4u a[N];
#pragma unroll
    for (int i = 0; i < N; ++i) a[i] = ldws(rs, off + i * STEP);
    unsigned d = 0;
#pragma unroll
    for (int i = 0; i < N; ++i) {
      a[i].x ^= T.t0, a[i].y ^= T.t1, a[i].z ^= T.t2, a[i].w ^= T.t3;
      d |= (a[i].x | a[i].y) | (a[i].z | a[i].w);
      v[i] = __builtin_bit_cast(f32x4, a[i]);
    }
    d &= MASK;
    if (ZW) d |= a[0].z | a[0].w;
    if (__builtin_expect(__builtin_amdgcn_ballot_w64(d != 0u) == 0ull, 1)) break;
    if (++spins > SPIN_CAP) break;
  }
}
__device__ __forceinline__ __amdgpu_buffer_rsrc_t mkrs(const void *p, int bytes) {
  return __builtin_amdgcn_make_buffer_rsrc((void *)p, 0, bytes, 0x00020000);
}
template <int AUX, int IMM = 0> __device__ __forceinline__ void dma1(__amdgpu_buffer_rsrc_t rs, LAS unsigned char *dst, int voff, int soff) {
  __builtin_amdgcn_raw_ptr_buffer_load_lds(rs, (LAS void *)dst, 16, voff, soff, IMM, AUX);
}
template <int W> __device__ __forceinline__ void dmal(__amdgpu_buffer_rsrc_t rs, LAS unsigned char *&dst, int voff, int &soff, int sstep, int n) {
#pragma unroll 1
  for (int i = 0; i < n; ++i) {
    if (W < 64) VMWAIT(W - 1);
    dma1<AUXW>(rs, dst, voff, soff);
    dst += 1024, soff += sstep;
  }
}
template <int K> __device__ __forceinline__ void arrive(int sa, unsigned one = 1u) {
  asm volatile("ds_add_u32 %0, %1 offset:%2" ::"v"(sa), "v"(one), "n"(4 * K) : "memory");
}
template <int K, int N, int SLEEP = 0> __device__ __forceinline__ void await(LAS unsigned char *syn) {
  while (__builtin_amdgcn_readfirstlane(*(volatile LAS unsigned *)(syn + 4 * K)) < (unsigned)N) {
    if (SLEEP) __builtin_amdgcn_s_sleep(SLEEP);
  }
  asm volatile("" ::: "memory");
}
#define ST(i)
#define STOUT()
}

__global__ void __launch_bounds__(NTHR, 3)
    blk_kernel(const u16 *x, const u16 *wq, const u16 *kc, const u16 *vc, const u16 *wo, const u16 *wg, const u16 *wu, const u16 *wd, u16 *out,
        unsigned char *ws) {
  extern __shared__ __attribute__((aligned(16))) unsigned char smem_[];
  LAS unsigned char *L = (LAS unsigned char *)smem_;
  u64 pc;
  asm volatile("s_getpc_b64 %0" : "=s"(pc));
  const int tid = threadIdx.x, l = tid & 63, wv = __builtin_amdgcn_readfirstlane(tid >> 6);
  const int c = blockIdx.x, xcd = c & 7, j = c >> 3;
  const int kq = j & 3;
  const int cg64 = (j >> 2) * 8 + xcd;
  const int ab = xcd, akv = j >> 3, as = j & 7;
  const int ku = 4 * xcd + (j >> 3), ocg = j & 7;
  const bool isG = j < 28;
  const int fg = xcd * 7 + (j >> 2);
  int sa = l == 0 ? L_SYNC : L_JUNK + 4 * l;

  const int lw = wv & 3;
  asm volatile("ds_write_b32 %0, %1" ::"v"(L_SYNC + 4 * (l & 15)), "v"(0u) : "memory");
  const int v128 = ((l & 7) ^ (((l >> 4) & 3) << 1)) * 16;
  int vx = l * 16, vq = (l >> 3) * 8192 + v128;
  int so = (kq * 256 + lw * 64) * 8192 + cg64 * 128, sx = lw * 4096;
  LAS unsigned char *d = L + L_R0 + lw * 8192, *dx = L + L_X + lw * 4096;
  asm volatile("" : "+v"(vx), "+v"(vq), "+s"(so), "+s"(sx), "+s"(d), "+s"(dx));
  asm volatile("s_waitcnt lgkmcnt(0)\n\ts_barrier" ::"s"(x), "s"(wq), "s"(kc), "s"(vc), "s"(wo), "s"(wg), "s"(wu), "s"(wd), "s"(out), "s"(ws)
               : "memory");
  if (__builtin_expect(wv < NLW, 1)) {
    {
      const __amdgpu_buffer_rsrc_t rx = mkrs(x, 8 * 1024 * 2);
      dma1<0, 0>(rx, dx, vx, sx);
      dma1<0, 1024>(rx, dx, vx, sx);
      dma1<0, 2048>(rx, dx, vx, sx);
      dma1<0, 3072>(rx, dx, vx, sx);
    }
    dmal<64>(mkrs(wq, 1024 * 4096 * 2), d, vq, so, 8 * 8192, 8);
    __builtin_amdgcn_raw_ptr_buffer_load_lds(mkrs(ws, 4096), (LAS void *)(L + L_EP), 4, 0, 0, 0, SC1);
    ST(1);
    VMWAIT(9);
    arrive<C_X>(sa);
    ST(2);
    const int kvbase = ((ab * 4 + akv) * 4096 + 3072 + as * 128 + lw * 32) * 256;
    const int r4 = l >> 4;
    const int v256 = ((l & 15) ^ ((4 * (lw & 1) + r4) << 1)) * 16;
    {
      const __amdgpu_buffer_rsrc_t rk = mkrs(kc, 8 * 4 * 4096 * 128 * 2);
      const int vk = r4 * 256 + (((l & 15) ^ (4 * lw + r4)) * 16);
      d += 32768 - 8192, so = kvbase;
      dmal<WIN>(rk, d, vk, so, 1024, WIN);
      arrive<C_Q>(sa);
      ST(3);
      dmal<WIN>(rk, d, vk, so, 1024, 8 - WIN);
    }
    {
      const __amdgpu_buffer_rsrc_t rv = mkrs(vc, 8 * 4 * 4096 * 128 * 2);
      d += 32768 - 8192, so = kvbase;
      dmal<WIN>(rv, d, r4 * 256 + v256, so, 1024, WIN);
      arrive<C_K>(sa);
      ST(4);
      dmal<WIN>(rv, d, r4 * 256 + v256, so, 1024, 8 - WIN);
    }
    {
      const __amdgpu_buffer_rsrc_t ro = mkrs(wo, 4096 * 1024 * 2);
      so = ((ku >> 1) * 256 + (ku & 1) * 64 + 4 * lw) * 2048 + ocg * 256;
      d += 32768 - 8192;
#pragma unroll 1
      for (int i = 0; i < 8; ++i) {
        VMWAIT(WIN - 1);
        if (i == WIN - 1) arrive<C_V>(sa);
        dma1<AUXW>(ro, d + i * 1024, r4 * 2048 + v256, so + ((i & 1) * 128 + (i >> 1) * 16) * 2048);
      }
    }
    ST(5);
    VMWAIT(0);
    arrive<C_WO>(sa);
    ST(6);
    BAR();
    ST(7);
    if (!isG) {
      STOUT();
      return;
    }
    const int sg = (kq * 256 + lw * 64) * 7168 + fg * 128;
    d = L + L_R0 + lw * 8192, so = sg;
    dmal<WIN2>(mkrs(wg, 1024 * 3584 * 2), d, (l >> 3) * 7168 + v128, so, 8 * 7168, 8);
    d += 32768 - 8192, so = sg;
    dmal<WIN2>(mkrs(wu, 1024 * 3584 * 2), d, (l >> 3) * 7168 + v128, so, 8 * 7168, 8);
    ST(8);
    {
      d += 32768 - 8192, so = (fg * 64 + 2 * lw) * 2048 + kq * 512;
      dmal<WIN2>(mkrs(wd, 3584 * 1024 * 2), d, (l >> 5) * 2048 + (((l & 31) ^ ((2 * lw + (l >> 5)) << 1)) * 16), so, 8 * 2048, 8);
    }
    ST(9);
    VMWAIT(H_TAIL);
    arrive<C_H>(sa);
    ST(10);
    VMWAIT(0);
    ST(11);
    STOUT();
    return;
  }

  const int w = wv - NLW;
  unsigned pf0, pf1, pf2;
  {
    const u64 cb = pc & ~255ull;
    const int o0 = (w + 24 * kq) * 128;
    asm volatile("s_load_dword %0, %3, %4\n\ts_load_dword %1, %3, %5\n\ts_load_dword %2, %3, %6"
                 : "=&s"(pf0), "=&s"(pf1), "=&s"(pf2)
                 : "s"(cb), "s"(min(o0, CODE_BYTES - 4)), "s"(min(o0 + 1024, CODE_BYTES - 4)), "s"(min(o0 + 2048, CODE_BYTES - 4)));
  }
  const int g = l >> 4, col = l & 15, b8 = l & 7, q4 = (l >> 2) & 3, p4 = l & 3;
  const int lst = (l & 1) * 64 + g * 16;
  const int dead = col < 8 ? 0 : OOB;
  const int trow = 4 * g + q4;
  const int sw128 = ((trow >> 1) & 3) << 1, sw256 = (trow & 7) << 1;
  LAS unsigned char *hw = L + L_ACT + (l >> 3) * 512 + (l & 3) * 128 + w * 16 + ((l >> 2) & 1) * 8;
  LAS unsigned char *bact = L + L_ACT + g * 128 + b8 * 16;
  LAS unsigned char *a128 = L + trow * 128 + (p4 & 1) * 8 + (((2 * (w & 3) + (p4 >> 1)) ^ sw128) * 16) + (w >> 2) * 32768;
  LAS unsigned char *syn = L + L_SYNC;
  int o_qs = WS_QP + (((((cg64 >> 4) * 4 + (b8 >> 1)) * 8 + ((cg64 >> 1) & 7)) * 4 + kq) * 8 + (cg64 & 1) * 4 + w) * 128 + lst + dead;
  LAS unsigned char *xa_ = L + L_X + w * 2048 + l * 16, *xo_ = L + L_X + w * 2048 + kq * 512 + l * 8;
  unsigned one = 1u;
  Tag T, T4;
  T.m = 0xffu, T4.m = 0xfu;
  const __amdgpu_buffer_rsrc_t rs = mkrs(ws, WS_END);
  asm volatile("" : "+v"(xa_), "+v"(xo_), "+v"(hw), "+v"(bact), "+v"(a128), "+v"(syn), "+v"(o_qs), "+v"(one), "+v"(T4.m), "+v"(sa));
  asm volatile("s_waitcnt lgkmcnt(0)" ::"s"(pf0), "s"(pf1), "s"(pf2));
  await<C_X, NLW>(syn);
  ST(1);
  {
    const v4u xa = *(LAS v4u *)xa_, xb = *(LAS v4u *)(xa_ + 1024);
    const v2u xo = *(LAS v2u *)xo_;
    f32x2 v[8];
#pragma unroll
    for (int i = 0; i < 4; ++i) {
      v[i] = f32x2{blo(xa[i]), bhi(xa[i])};
      v[4 + i] = f32x2{blo(xb[i]), bhi(xb[i])};
    }
    const f32x2 s = ((v[0] + v[1]) + (v[2] + v[3])) + ((v[4] + v[5]) + (v[6] + v[7]));
    const float mean = sum64(s.x + s.y) * (1.f / 1024.f);
    f32x2 qq = {0.f, 0.f};
#pragma unroll
    for (int i = 0; i < 8; ++i) qq += (v[i] - mean) * (v[i] - mean);
    const float var = rbf(sum64(qq.x + qq.y) * (1.f / 1024.f));
    const float m = rbf(mean), ri = __builtin_amdgcn_rcpf(rbf(__builtin_amdgcn_sqrtf(rbf(var + 1e-5f))));
    const unsigned d0 = pkbf(blo(xo.x) - m, bhi(xo.x) - m), d1 = pkbf(blo(xo.y) - m, bhi(xo.y) - m);
    *(LAS v2u *)hw = v2u{pkbf(blo(d0) * ri, bhi(d0) * ri), pkbf(blo(d1) * ri, bhi(d1) * ri)};
  }
  arrive<C_XN>(sa, one);
  ST(2);
  unsigned Ev;
  if (w < 4) {
    await<C_XN, NCW>(syn);
    v8s af[8];
    v4u bf[8];
    rd_b<8>(bact, bf);
    await<C_Q, NLW>(syn);
    Ev = *(LAS unsigned *)(syn + (L_EP - L_SYNC));
    rd_a<8, 4096>(a128, af);
    SCHED();
    const unsigned E0 = __builtin_amdgcn_readfirstlane(Ev);
    T4.t0 = E0 + 1u, T4.t1 = (E0 >> 8) + 1u, T4.t2 = (E0 >> 16) + 1u, T4.t3 = (E0 >> 24) + 1u;
    stws(rs, o_qs, tag4(mm<8>(af, bf), T4));
  } else {
    await<C_Q, NLW, 2>(syn);
    Ev = *(LAS unsigned *)(syn + (L_EP - L_SYNC));
  }
  ST(3);
  const unsigned E = __builtin_amdgcn_readfirstlane(Ev);
  const unsigned Tw = ((E & 0x7f7f7f7fu) + 0x01010101u) ^ (E & 0x80808080u);
  T.t0 = Tw & 0xffu, T.t1 = (Tw >> 8) & 0xffu, T.t2 = (Tw >> 16) & 0xffu, T.t3 = Tw >> 24;
  T4.t0 = T.t0 & 0xfu, T4.t1 = T.t1 & 0xfu, T4.t2 = T.t2 & 0xfu, T4.t3 = T.t3 & 0xfu;
  SCHED();
  const int rr = xcd * 4 + kq, rb = 2 * (rr >> 3) + ((l >> 2) & 1), rcol = (rr & 7) * 128 + 32 * (w & 3) + 16 * ((l >> 3) & 1) + 4 * p4;
  const int dcu = xcd * 28 + j, eu = (w == 0) ? dcu : 224 + dcu;
  const bool hasE = (w == 0) || (w == 1 && dcu < 32);
  const int drow = 16 * ((trow >> 1) & 3) + 2 * (g >> 1) + (trow & 1);
  LAS unsigned char *a256 = L + L_R2 + (32 * g + q4) * 256 + (p4 & 1) * 8 + (((2 * w + (p4 >> 1)) ^ sw256) * 16);
  LAS unsigned char *kp[4];
#pragma unroll
  for (int i = 0; i < 4; ++i) kp[i] = L + L_R1 + (32 * q4 + 4 * w + p4) * 256 + (((4 * i + g) ^ col) * 16);
  LAS unsigned char *qw = L + L_ACT2 + ((l >> 3) & 3) * 512 + ((l >> 1) & 3) * 128 + w * 16 + (l & 1) * 8 + (l >> 5) * 4;
  LAS unsigned char *mxw = L + (l < 8 ? L_MX + l * 32 + w * 4 : L_JUNK + 256 + 4 * l), *mxr = L + L_MX + b8 * 32;
  LAS unsigned char *pw = col < 8 ? bact + (w >> 1) * 512 + (w & 1) * 8 : L + L_JUNK + 1024 + 8 * l;
  LAS unsigned char *ow = L + L_ACT2 + ((l >> 3) & 3) * 512 + p4 * 128 + w * 16 + ((l >> 2) & 1) * 8 + (l >> 5) * 4;
  LAS unsigned char *dw = L + L_ACT2 + (w & 1) * 512 + p4 * 128 + (2 * (w >> 1) + ((l >> 2) & 1)) * 16 + (col >> 3) * 8 + g * 2;
  LAS unsigned char *ad0 = L + L_R2 + drow * 512 + (p4 & 1) * 8 + (((4 * w + (p4 >> 1)) ^ sw256) * 16);
  LAS unsigned char *ad1 = L + L_R2 + drow * 512 + (p4 & 1) * 8 + (((4 * w + 2 + (p4 >> 1)) ^ sw256) * 16);
  int o_qg = WS_QP + ((akv * 4 + (ab >> 1)) * 8 + w) * 4096 + (l >> 2) * 128 + (ab & 1) * 64 + p4 * 16;
  int o_as = WS_AP + (((akv * 8 + (b8 >> 1) * 2 + (w >> 2)) * 64 + ab * 8 + as) * 4 + (w & 3)) * 128 + lst + dead;
  int o_ms = WS_ML + (akv * 64 + ab * 8 + as) * 128 + l * 16 + (l < 8 ? 0 : OOB);
  int o_mg = WS_ML + ((ku >> 3) * 64 + w * 8 + 4 * (l >> 5) + p4) * 128 + (2 * ((ku >> 1) & 3) + ((l >> 2) & 1)) * 16;
  int o_ag = WS_AP + (ku * 64 + w * 8 + 4 * (l >> 5)) * 512 + (l & 31) * 16;
  int o_os = WS_OP + ((((b8 >> 1) * 32 + ocg * 4 + (w >> 1)) * 32 + ku) * 2 + (w & 1)) * 128 + lst + dead;
  int o_s2 = isG ? WS_GP + ((fg * 4 + kq) * 8 + w) * 512 + b8 * 64 + g * 16 + dead : WS_HB + (rb * 1024 + rcol) * 4 + (l < 16 && w < 4 ? 0 : OOB);
  int o_g2 = isG ? WS_HB + w * 4096 + l * 16 : WS_OP + (rr * 4 + w) * 8192 + l * 16;
  int o_ds = isG ? WS_DP + ((b8 * 32 + 8 * kq + w) * 56 + fg) * 128 + (col >> 3) * 64 + g * 16 : OOB;
  int o_gg = WS_GP + ((fg * 4 + g) * 8 + 2 * (w & 1) + (col >> 3)) * 512 + (w >> 1) * 128 + b8 * 16;
  int o_eh = WS_HB + eu * 128 + b8 * 16, o_dg = WS_DP + eu * 7168 + l * 16, o_out = (eu * 32 + 4 * b8 + g) * 2 + dead;
  v2u xr = *(LAS v2u *)(L + L_X + (rb * 1024 + rcol) * 2);
  asm volatile("" : "+v"(xr), "+v"(a256), "+v"(kp[0]), "+v"(kp[1]), "+v"(kp[2]), "+v"(kp[3]), "+v"(qw), "+v"(mxw), "+v"(mxr), "+v"(pw), "+v"(ow),
               "+v"(dw), "+v"(ad0), "+v"(ad1));
  asm volatile("" : "+v"(o_qg), "+v"(o_as), "+v"(o_ms), "+v"(o_mg), "+v"(o_ag), "+v"(o_os), "+v"(o_s2), "+v"(o_g2), "+v"(o_ds), "+v"(o_gg),
               "+v"(o_eh), "+v"(o_dg), "+v"(o_out), "+v"(T.m));
  const v4u bad = {~T.t0, ~T.t1, ~T.t2, ~T.t3};
  stws(rs, o_as, bad);
  await<C_K, NLW, 1>(syn);
  __builtin_amdgcn_s_sleep(DLY_Q);
  float M;
  v4u pf[4];
  {
    v8s kf[4];
#pragma unroll
    for (int i = 0; i < 4; ++i) kf[i] = *(LAS v8s *)kp[i];
    f32x4 a[2];
    gather<2, 2048, 0xfu>(rs, o_qg, a, T4);
    ST(4);
    const f32x4 s = a[0] + a[1];
    *(LAS unsigned *)qw = pkbf(rs32(s.x, s.z), rs32(s.y, s.w));
    arrive<C_QL>(sa, one);
    await<C_QL, NCW>(syn);
    ST(5);
    v4u qf[4];
    rd_b<4>(bact + 4096, qf);
    const f32x4 acc = mm<4>(kf, qf);
    const unsigned r0 = pkbf(acc.x, acc.y), r1 = pkbf(acc.z, acc.w);
    const float SCL = 0.08838834764831845f;
    const unsigned s0 = pkbf(blo(r0) * SCL, bhi(r0) * SCL), s1 = pkbf(blo(r1) * SCL, bhi(r1) * SCL);
    const float sc0 = blo(s0), sc1 = bhi(s0), sc2 = blo(s1), sc3 = bhi(s1);
    *(LAS float *)mxw = mx32(mx16(fmx(fmx(sc0, sc1), fmx(sc2, sc3))));
    arrive<C_MX>(sa, one);
    await<C_MX, NCW>(syn);
    const f32x4 m0 = *(LAS f32x4 *)mxr, m1 = *(LAS f32x4 *)(mxr + 16);
    M = fmx(fmx(fmx(m0.x, m0.y), fmx(m0.z, m0.w)), fmx(fmx(m1.x, m1.y), fmx(m1.z, m1.w)));
    const float p0 = __builtin_amdgcn_exp2f((sc0 - M) * LOG2E), p1 = __builtin_amdgcn_exp2f((sc1 - M) * LOG2E),
                p2 = __builtin_amdgcn_exp2f((sc2 - M) * LOG2E), p3 = __builtin_amdgcn_exp2f((sc3 - M) * LOG2E);
    *(LAS v2u *)pw = v2u{pkbf(p0, p1), pkbf(p2, p3)};
    float ls = (p0 + p1) + (p2 + p3);
    ls = rs16(ls, ls);
    *(LAS float *)(mxw + 256) = rs32(ls, ls);
    arrive<C_P>(sa, one);
    await<C_P, NCW>(syn);
    rd_b<4>(bact, pf);
    if (w == 7) {
      const f32x4 l2 = *(LAS f32x4 *)(mxr + 256) + *(LAS f32x4 *)(mxr + 272);
      stws(rs, o_ms, tag4(f32x4{M, (l2.x + l2.y) + (l2.z + l2.w), 0.f, 0.f}, T));
    }
  }
  ST(6);
  await<C_V, NLW>(syn);
  {
    v8s af[4];
    rd_a<4, 2048>(a256, af);
    stws(rs, o_as, tag4(mm<4>(af, pf), T));
  }
  ST(7);
  stws(rs, o_os, bad);
  stws(rs, o_s2, bad);
  stws(rs, o_ds, bad);
  {
    f32x4 ml[1];
    gather<1, 0, 0xffu, true>(rs, o_mg, ml, T);
    float mm_ = fmx(ml[0].x, dpp<XOR1>(ml[0].x));
    mm_ = mx32(fmx(mm_, dpp<XOR2>(mm_)));
    const float wt = __builtin_amdgcn_exp2f((ml[0].x - mm_) * LOG2E);
    float ll = wt * ml[0].y;
    ll += dpp<XOR1>(ll);
    ll += dpp<XOR2>(ll);
    ll = rs32(ll, ll);
    const float cf = wt * __builtin_amdgcn_rcpf(ll);
    float c0 = dpp<0x00>(cf), c1 = dpp<0x55>(cf), c2 = dpp<0xAA>(cf), c3 = dpp<0xFF>(cf);
    asm volatile("" : "+v"(c0), "+v"(c1), "+v"(c2), "+v"(c3));
    await<C_WO, NLW>(syn);
    ST(8);
    v8s af[4];
    rd_a<4, 2048>(a256 + 32768, af);
    f32x4 a[4];
    gather<4, 512>(rs, o_ag, a, T);
    ST(9);
    const f32x4 o = (a[0] * c0 + a[1] * c1) + (a[2] * c2 + a[3] * c3);
    *(LAS unsigned *)ow = pkbf(rs32(o.x, o.z), rs32(o.y, o.w));
    BAR();
    v4u bf[4];
    rd_b<4>(bact + 4096, bf);
    stws(rs, o_os, tag4(mm<4>(af, bf), T));
  }
  ST(10);
  if (!isG) {
    if (w < 4) {
      __builtin_amdgcn_s_sleep(DLY_R);
      f32x4 a[8];
      gather<8, 1024>(rs, o_g2, a, T);
      ST(11);
      f32x4 s = ((a[0] + a[1]) + (a[2] + a[3])) + ((a[4] + a[5]) + (a[6] + a[7]));
#pragma unroll
      for (int i = 0; i < 4; ++i) {
        const float t = rs16(s[i], s[i]);
        s[i] = rs32(t, t);
      }
      const unsigned t0 = pkbf(s.x, s.y), t1 = pkbf(s.z, s.w);
      const unsigned h0 = pkbf(blo(xr.x) + blo(t0), bhi(xr.x) + bhi(t0)), h1 = pkbf(blo(xr.y) + blo(t1), bhi(xr.y) + bhi(t1));
      stws(rs, o_s2, v4u{(h0 << 16) | T.t0, (h0 & 0xffff0000u) | T.t1, (h1 << 16) | T.t2, (h1 & 0xffff0000u) | T.t3});
      ST(12);
    }
  } else {
    await<C_H, NLW>(syn);
    {
      v8s af[8];
      rd_a<8, 4096>(a128, af);
      f32x4 h[4];
      gather<4, 1024>(rs, o_g2, h, T);
      ST(11);
      const f32x4 sv = (h[0] + h[1]) + (h[2] + h[3]);
      const float mean = sum64((sv.x + sv.y) + (sv.z + sv.w)) * (1.f / 1024.f);
      const f32x4 d0 = h[0] - mean, d1 = h[1] - mean, d2 = h[2] - mean, d3 = h[3] - mean;
      const f32x4 qv = (d0 * d0 + d1 * d1) + (d2 * d2 + d3 * d3);
      const float var = rbf(sum64((qv.x + qv.y) + (qv.z + qv.w)) * (1.f / 1024.f));
      const float m = rbf(mean), ri = __builtin_amdgcn_rcpf(rbf(__builtin_amdgcn_sqrtf(rbf(var + 1e-5f))));
      const f32x4 ho = (kq & 2) ? ((kq & 1) ? h[3] : h[2]) : ((kq & 1) ? h[1] : h[0]);
      const unsigned e0 = pkbf(ho.x - m, ho.y - m), e1 = pkbf(ho.z - m, ho.w - m);
      *(LAS v2u *)hw = v2u{pkbf(blo(e0) * ri, bhi(e0) * ri), pkbf(blo(e1) * ri, bhi(e1) * ri)};
      BAR();
      ST(12);
      v4u bf[8];
      rd_b<8>(bact, bf);
      stws(rs, o_s2, tag4(mm<8>(af, bf), T));
    }
    if (c == 0 && w == 2 && l == 0) __hip_atomic_store((GAS unsigned *)ws, Tw, __ATOMIC_RELAXED, __HIP_MEMORY_SCOPE_AGENT);
    float hv = 0.f;
    if (hasE) {
      f32x4 hE[1];
      gather<1, 0>(rs, o_eh, hE, T);
      hv = (g & 2) ? ((g & 1) ? hE[0].w : hE[0].z) : ((g & 1) ? hE[0].y : hE[0].x);
    }
    __builtin_amdgcn_s_sleep(DLY_D);
    {
      v8s a0[2], a1[2];
      rd_a<2, 4096>(ad0, a0);
      rd_a<2, 4096>(ad1, a1);
      f32x4 a[2];
      gather<2, 2048>(rs, o_gg, a, T);
      ST(13);
      const unsigned gu = pkbf(rs16(rs32(a[0].x, a[0].z), rs32(a[0].y, a[0].w)), rs16(rs32(a[1].x, a[1].z), rs32(a[1].y, a[1].w)));
      const float sg = rbf(__builtin_amdgcn_rcpf(1.f + __builtin_amdgcn_exp2f(blo(gu) * -LOG2E)));
      *(LAS u16 *)dw = (u16)pkbf(sg * bhi(gu), 0.f);
      BAR();
      v4u bf[2];
      rd_b<2>(bact + 4096, bf);
      SCHED();
      f32x4 c0 = {0.f, 0.f, 0.f, 0.f}, c1 = {0.f, 0.f, 0.f, 0.f};
      c0 = mfma(a0[0], bf[0], c0);
      c1 = mfma(a1[0], bf[0], c1);
      c0 = mfma(a0[1], bf[1], c0);
      c1 = mfma(a1[1], bf[1], c1);
      stws(rs, o_ds, tag4(col < 8 ? c0 : c1, T));
    }
    ST(14);
    if (hasE) {
      __builtin_amdgcn_s_sleep(DLY_E);
      f32x4 a[7];
      gather<7, 1024>(rs, o_dg, a, T);
      const f32x4 s = ((a[0] + a[1]) + (a[2] + a[3])) + ((a[4] + a[5]) + a[6]);
      float t = rs16(rs32(s.x, s.z), rs32(s.y, s.w));
      t += dpp<XOR8>(t);
      __builtin_amdgcn_raw_buffer_store_b16((u16)pkbf(hv + rbf(t), 0.f), mkrs(out, 8 * 1024 * 2), o_out, 0, 0);
    }
  }
  STOUT();
}

extern "C" void kernel_launch(void *const *d_in, const int *in_sizes, int n_in, void *d_out, int out_size, void *d_ws, size_t ws_size,
    hipStream_t stream) {
  (void)in_sizes, (void)n_in, (void)out_size, (void)ws_size;
  (void)hipFuncSetAttribute((const void *)blk_kernel, hipFuncAttributeMaxDynamicSharedMemorySize, L_END);
  blk_kernel<<<256, NTHR, L_END, stream>>>((const u16 *)d_in[0], (const u16 *)d_in[3], (const u16 *)d_in[1], (const u16 *)d_in[2],
      (const u16 *)d_in[4], (const u16 *)d_in[6], (const u16 *)d_in[7], (const u16 *)d_in[8], (u16 *)d_out, (unsigned char *)d_ws);
}
```

```cpp
#include <hip/hip_runtime.h>

#define GAS __attribute__((address_space(1)))
#define LAS __attribute__((address_space(3)))
typedef unsigned short u16;
typedef unsigned long long u64;
typedef unsigned v4u __attribute__((ext_vector_type(4)));
typedef unsigned v2u __attribute__((ext_vector_type(2)));
typedef float f32x4 __attribute__((ext_vector_type(4)));
typedef short v4s __attribute__((ext_vector_type(4)));
typedef short v8s __attribute__((ext_vector_type(8)));
typedef __bf16 v8bf __attribute__((ext_vector_type(8)));
typedef __bf16 v2bf __attribute__((ext_vector_type(2)));
typedef float f32x2 __attribute__((ext_vector_type(2)));

#define AUXW 2
#define WIN 6
#define EPOFF 0
#define WIN2 8
#define DLY_Q 0
#define DLY_R 12
#define DLY_D 8
#define DLY_E 14
#define H_TAIL 3
#define WG_EARLY 0
#define PREW 3
#define WARM 1
#define CODE_BYTES 10496

namespace {
constexpr int NCW = 8, NLW = 4, NTHR = (NCW + NLW) * 64;
constexpr int WS_EPOCH = 0;
constexpr int WS_QP = 4096;
constexpr int WS_AP = WS_QP + 4 * 8 * 4096 * 4;
constexpr int WS_ML = WS_AP + 32 * 64 * 128 * 4;
constexpr int WS_OP = WS_ML + 32 * 64 * 16;
constexpr int WS_HB = WS_OP + 32 * 8 * 1024 * 4;
constexpr int WS_GP = WS_HB + 8 * 1024 * 4;
constexpr int WS_DP = WS_GP + 56 * 16384;
constexpr int WS_END = WS_DP + 56 * 8 * 1024 * 4;
constexpr int WS_STAMP = (WS_END + 4095) / 4096 * 4096;
constexpr int OOB = 0x40000000;
constexpr int L_R0 = 0, L_R1 = 32768, L_R2 = 65536, L_R3 = 98304, L_X = 131072, L_ACT = L_X + 16384, L_ACT2 = L_ACT + 4096,
              L_MX = L_ACT2 + 2048, L_LS = L_MX + 256, L_SYNC = L_LS + 256, L_EP = L_SYNC + 64, L_JUNK = L_EP + 256, L_END = L_JUNK + 4096;
enum { C_X = 0, C_XN, C_Q, C_K, C_QL, C_MX, C_P, C_V, C_WO, C_H, C_N };
constexpr int SC1 = 16;
constexpr unsigned SPIN_CAP = 1u << 16;
constexpr float LOG2E = 1.4426950408889634f;

#define BAR() asm volatile("s_waitcnt lgkmcnt(0)\n\ts_barrier" ::: "memory")
#define VMWAIT(n) asm volatile("s_waitcnt vmcnt(%0)" ::"n"(n) : "memory")
#define SCHED() __builtin_amdgcn_sched_barrier(0)
#define PIN(x) asm volatile("" : "+v"(x))

__device__ __forceinline__ float asf(unsigned u) { return __builtin_bit_cast(float, u); }
__device__ __forceinline__ unsigned asu(float f) { return __builtin_bit_cast(unsigned, f); }
__device__ __forceinline__ unsigned pkbf(float lo, float hi) {
  f32x2 v = {lo, hi};
  return __builtin_bit_cast(unsigned, __builtin_convertvector(v, v2bf));
}
__device__ __forceinline__ float blo(unsigned p) { return asf(p << 16); }
__device__ __forceinline__ float bhi(unsigned p) { return asf(p & 0xffff0000u); }
__device__ __forceinline__ float rbf(float f) { return blo(pkbf(f, f)); }
__device__ __forceinline__ float fmx(float a, float b) { return __builtin_fmaxf(a, b); }
template <int CTRL> __device__ __forceinline__ float dpp(float x) {
  return asf(__builtin_amdgcn_mov_dpp(asu(x), CTRL, 0xf, 0xf, true));
}
constexpr int XOR1 = 0xB1, XOR2 = 0x4E, XOR7 = 0x141, XOR8 = 0x128;
__device__ __forceinline__ float rs32(float a, float b) {
  auto r = __builtin_amdgcn_permlane32_swap(asu(a), asu(b), false, false);
  return asf(r[0]) + asf(r[1]);
}
__device__ __forceinline__ float rs16(float a, float b) {
  auto r = __builtin_amdgcn_permlane16_swap(asu(a), asu(b), false, false);
  return asf(r[0]) + asf(r[1]);
}
__device__ __forceinline__ float mx32(float a) {
  auto r = __builtin_amdgcn_permlane32_swap(asu(a), asu(a), false, false);
  return fmx(asf(r[0]), asf(r[1]));
}
__device__ __forceinline__ float mx16(float a) {
  auto r = __builtin_amdgcn_permlane16_swap(asu(a), asu(a), false, false);
  return fmx(asf(r[0]), asf(r[1]));
}
__device__ __forceinline__ float sum64(float x) {
  x += dpp<XOR1>(x);
  x += dpp<XOR2>(x);
  x += dpp<XOR7>(x);
  x += dpp<XOR8>(x);
  x = rs16(x, x);
  return rs32(x, x);
}
struct Tag {
  unsigned t0, t1, t2, t3;
  unsigned m;
};
__device__ __forceinline__ v4u tag4(f32x4 v, const Tag &T) {
  v4u o;
  o.x = (T.m & T.t0) | (~T.m & asu(v.x));
  o.y = (T.m & T.t1) | (~T.m & asu(v.y));
  o.z = (T.m & T.t2) | (~T.m & asu(v.z));
  o.w = (T.m & T.t3) | (~T.m & asu(v.w));
  return o;
}
__device__ __forceinline__ v4u ldws(__amdgpu_buffer_rsrc_t rs, int off) { return __builtin_amdgcn_raw_buffer_load_b128(rs, off, 0, SC1); }
__device__ __forceinline__ void stws(__amdgpu_buffer_rsrc_t rs, int off, v4u v) { __builtin_amdgcn_raw_buffer_store_b128(v, rs, off, 0, SC1); }
__device__ __forceinline__ f32x4 mfma(v8s a, v4u b, f32x4 c) {
  return __builtin_amdgcn_mfma_f32_16x16x32_bf16(__builtin_bit_cast(v8bf, a), __builtin_bit_cast(v8bf, b), c, 0, 0, 0);
}
__device__ __forceinline__ v8s trfrag(LAS unsigned char *p, int off0, int off1) {
  const v4s a = __builtin_amdgcn_ds_read_tr16_b64_v4i16((LAS v4s *)(p + off0));
  const v4s b = __builtin_amdgcn_ds_read_tr16_b64_v4i16((LAS v4s *)(p + off1));
  return __builtin_shufflevector(a, b, 0, 1, 2, 3, 4, 5, 6, 7);
}
template <int NK, int KS> __device__ __forceinline__ void rd_a(LAS unsigned char *a, v8s (&af)[NK]) {
#pragma unroll
  for (int ks = 0; ks < NK; ++ks) af[ks] = trfrag(a, ks * KS, ks * KS + KS / 2);
}
template <int NK> __device__ __forceinline__ void rd_b(LAS unsigned char *b, v4u (&bf)[NK]) {
#pragma unroll
  for (int ks = 0; ks < NK; ++ks) bf[ks] = *(LAS v4u *)(b + ks * 512);
}
template <int NK> __device__ __forceinline__ f32x4 mm(const v8s (&af)[NK], const v4u (&bf)[NK]) {
  SCHED();
  f32x4 acc = {0.f, 0.f, 0.f, 0.f};
#pragma unroll
  for (int ks = 0; ks < NK; ++ks) acc = mfma(af[ks], bf[ks], acc);
  return acc;
}
template <int NK> __device__ __forceinline__ void pin_a(v8s (&af)[NK]) {
#pragma unroll
  for (int ks = 0; ks < NK; ++ks) PIN(af[ks]);
}
template <int N, int STEP, unsigned MASK = 0xffu, bool ZW = false>
__device__ __forceinline__ void gather(__amdgpu_buffer_rsrc_t rs, int off, f32x4 (&v)[N], const Tag &T) {
  unsigned spins = 0;
#pragma unroll 1
  for (;;) {
    asm volatile("" ::: "memory");
    v4u a[N];
#pragma unroll
    for (int i = 0; i < N; ++i) a[i] = ldws(rs, off + i * STEP);
    unsigned d = 0;
#pragma unroll
    for (int i = 0; i < N; ++i) {
      a[i].x ^= T.t0, a[i].y ^= T.t1, a[i].z ^= T.t2, a[i].w ^= T.t3;
      d |= (a[i].x | a[i].y) | (a[i].z | a[i].w);
      v[i] = __builtin_bit_cast(f32x4, a[i]);
    }
    d &= MASK;
    if (ZW) d |= a[0].z | a[0].w;
    if (__builtin_expect(__builtin_amdgcn_ballot_w64(d != 0u) == 0ull, 1)) break;
    if (++spins > SPIN_CAP) break;
  }
}
__device__ __forceinline__ __amdgpu_buffer_rsrc_t mkrs(const void *p, int bytes) {
  return __builtin_amdgcn_make_buffer_rsrc((void *)p, 0, bytes, 0x00020000);
}
template <int AUX, int IMM = 0> __device__ __forceinline__ void dma1(__amdgpu_buffer_rsrc_t rs, LAS unsigned char *dst, int voff, int soff) {
  __builtin_amdgcn_raw_ptr_buffer_load_lds(rs, (LAS void *)dst, 16, voff, soff, IMM, AUX);
}
template <int W> __device__ __forceinline__ void dmal(__amdgpu_buffer_rsrc_t rs, LAS unsigned char *&dst, int voff, int &soff, int sstep, int n) {
#pragma unroll 1
  for (int i = 0; i < n; ++i) {
    if (W < 64) VMWAIT(W - 1);
    dma1<AUXW>(rs, dst, voff, soff);
    dst += 1024, soff += sstep;
  }
}
template <int K> __device__ __forceinline__ void arrive(int sa, unsigned one = 1u) {
  asm volatile("ds_add_u32 %0, %1 offset:%2" ::"v"(sa), "v"(one), "n"(4 * K) : "memory");
}
template <int K, int N, int SLEEP = 0> __device__ __forceinline__ void await(LAS unsigned char *syn) {
  while (__builtin_amdgcn_readfirstlane(*(volatile LAS unsigned *)(syn + 4 * K)) < (unsigned)N) {
    if (SLEEP) __builtin_amdgcn_s_sleep(SLEEP);
  }
  asm volatile("" ::: "memory");
}
#define ST(i)
#define STOUT()
}

__global__ void __launch_bounds__(NTHR, 3)
    blk_kernel(const u16 *x, const u16 *wq, const u16 *kc, const u16 *vc, const u16 *wo, const u16 *wg, const u16 *wu, const u16 *wd, u16 *out,
        unsigned char *ws) {
  extern __shared__ __attribute__((aligned(16))) unsigned char smem_[];
  LAS unsigned char *L = (LAS unsigned char *)smem_;
  u64 pc;
  asm volatile("s_getpc_b64 %0" : "=s"(pc));
  const int tid = threadIdx.x, l = tid & 63, wv = __builtin_amdgcn_readfirstlane(tid >> 6);
  const int c = blockIdx.x, xcd = c & 7, j = c >> 3;
  const int kq = j & 3;
  const int cg64 = (j >> 2) * 8 + xcd;
  const int ab = xcd, akv = j >> 3, as = j & 7;
  const int ku = 4 * xcd + (j >> 3), ocg = j & 7;
  const bool isG = j < 28;
  const int fg = xcd * 7 + (j >> 2);
  int sa = l == 0 ? L_SYNC : L_JUNK + 4 * l;

  const int lw = wv & 3;
  asm volatile("ds_write_b32 %0, %1" ::"v"(L_SYNC + 4 * (l & 15)), "v"(0u) : "memory");
  const int v128 = ((l & 7) ^ (((l >> 4) & 3) << 1)) * 16;
  int vx = l * 16, vq = (l >> 3) * 8192 + v128;
  int so = (kq * 256 + lw * 64) * 8192 + cg64 * 128, sx = lw * 4096;
  LAS unsigned char *d = L + L_R0 + lw * 8192, *dx = L + L_X + lw * 4096;
  asm volatile("" : "+v"(vx), "+v"(vq), "+s"(so), "+s"(sx), "+s"(d), "+s"(dx));
  asm volatile("s_waitcnt lgkmcnt(0)\n\ts_barrier" ::"s"(x), "s"(wq), "s"(kc), "s"(vc), "s"(wo), "s"(wg), "s"(wu), "s"(wd), "s"(out), "s"(ws)
               : "memory");
  if (__builtin_expect(wv < NLW, 1)) {
    {
      const __amdgpu_buffer_rsrc_t rx = mkrs(x, 8 * 1024 * 2);
      dma1<0, 0>(rx, dx, vx, sx);
      dma1<0, 1024>(rx, dx, vx, sx);
      dma1<0, 2048>(rx, dx, vx, sx);
      dma1<0, 3072>(rx, dx, vx, sx);
    }
    dmal<64>(mkrs(wq, 1024 * 4096 * 2), d, vq, so, 8 * 8192, 8);
    __builtin_amdgcn_raw_ptr_buffer_load_lds(mkrs(ws, 4096), (LAS void *)(L + L_EP), 4, 0, 0, 0, SC1);
    ST(1);
    VMWAIT(9);
    arrive<C_X>(sa);
    ST(2);
    const int kvbase = ((ab * 4 + akv) * 4096 + 3072 + as * 128 + lw * 32) * 256;
    const int r4 = l >> 4;
    const int v256 = ((l & 15) ^ ((4 * (lw & 1) + r4) << 1)) * 16;
    {
      const __amdgpu_buffer_rsrc_t rk = mkrs(kc, 8 * 4 * 4096 * 128 * 2);
      const int vk = r4 * 256 + (((l & 15) ^ (4 * lw + r4)) * 16);
      d += 32768 - 8192, so = kvbase;
      dmal<WIN>(rk, d, vk, so, 1024, WIN);
      arrive<C_Q>(sa);
      ST(3);
      dmal<WIN>(rk, d, vk, so, 1024, 8 - WIN);
    }
    {
      const __amdgpu_buffer_rsrc_t rv = mkrs(vc, 8 * 4 * 4096 * 128 * 2);
      d += 32768 - 8192, so = kvbase;
      dmal<WIN>(rv, d, r4 * 256 + v256, so, 1024, WIN);
      arrive<C_K>(sa);
      ST(4);
      dmal<WIN>(rv, d, r4 * 256 + v256, so, 1024, 8 - WIN);
    }
    {
      const __amdgpu_buffer_rsrc_t ro = mkrs(wo, 4096 * 1024 * 2);
      so = ((ku >> 1) * 256 + (ku & 1) * 64 + 4 * lw) * 2048 + ocg * 256;
      d += 32768 - 8192;
#pragma unroll 1
      for (int i = 0; i < 8; ++i) {
        VMWAIT(WIN - 1);
        if (i == WIN - 1) arrive<C_V>(sa);
        dma1<AUXW>(ro, d + i * 1024, r4 * 2048 + v256, so + ((i & 1) * 128 + (i >> 1) * 16) * 2048);
      }
    }
    ST(5);
    VMWAIT(0);
    arrive<C_WO>(sa);
    ST(6);
    BAR();
    ST(7);
    if (!isG) {
      STOUT();
      return;
    }
    const int sg = (kq * 256 + lw * 64) * 7168 + fg * 128;
    d = L + L_R0 + lw * 8192, so = sg;
    dmal<WIN2>(mkrs(wg, 1024 * 3584 * 2), d, (l >> 3) * 7168 + v128, so, 8 * 7168, 8);
    d += 32768 - 8192, so = sg;
    dmal<WIN2>(mkrs(wu, 1024 * 3584 * 2), d, (l >> 3) * 7168 + v128, so, 8 * 7168, 8);
    ST(8);
    {
      d += 32768 - 8192, so = (fg * 64 + 2 * lw) * 2048 + kq * 512;
      dmal<WIN2>(mkrs(wd, 3584 * 1024 * 2), d, (l >> 5) * 2048 + (((l & 31) ^ ((2 * lw + (l >> 5)) << 1)) * 16), so, 8 * 2048, 8);
    }
    ST(9);
    VMWAIT(H_TAIL);
    arrive<C_H>(sa);
    ST(10);
    VMWAIT(0);
    ST(11);
    STOUT();
    return;
  }

  const int w = wv - NLW;
  unsigned pf0, pf1, pf2;
  {
    const u64 cb = pc & ~255ull;
    const int o0 = (w + 24 * kq) * 128;
    asm volatile("s_load_dword %0, %3, %4\n\ts_load_dword %1, %3, %5\n\ts_load_dword %2, %3, %6"
                 : "=&s"(pf0), "=&s"(pf1), "=&s"(pf2)
                 : "s"(cb), "s"(min(o0, CODE_BYTES - 4)), "s"(min(o0 + 1024, CODE_BYTES - 4)), "s"(min(o0 + 2048, CODE_BYTES - 4)));
  }
  const int g = l >> 4, col = l & 15, b8 = l & 7, q4 = (l >> 2) & 3, p4 = l & 3;
  const int lst = (l & 1) * 64 + g * 16;
  const int dead = col < 8 ? 0 : OOB;
  const int trow = 4 * g + q4;
  const int sw128 = ((trow >> 1) & 3) << 1, sw256 = (trow & 7) << 1;
  LAS unsigned char *hw = L + L_ACT + (l >> 3) * 512 + (l & 3) * 128 + w * 16 + ((l >> 2) & 1) * 8;
  LAS unsigned char *bact = L + L_ACT + g * 128 + b8 * 16;
  LAS unsigned char *a128 = L + trow * 128 + (p4 & 1) * 8 + (((2 * (w & 3) + (p4 >> 1)) ^ sw128) * 16) + (w >> 2) * 32768;
  LAS unsigned char *syn = L + L_SYNC;
  int o_qs = WS_QP + (((((cg64 >> 4) * 4 + (b8 >> 1)) * 8 + ((cg64 >> 1) & 7)) * 4 + kq) * 8 + (cg64 & 1) * 4 + w) * 128 + lst + dead;
  LAS unsigned char *xa_ = L + L_X + w * 2048 + l * 16, *xo_ = L + L_X + w * 2048 + kq * 512 + l * 8;
  unsigned one = 1u;
  Tag T, T4;
  T.m = 0xffu, T4.m = 0xfu;
  const __amdgpu_buffer_rsrc_t rs = mkrs(ws, WS_END);
  asm volatile("" : "+v"(xa_), "+v"(xo_), "+v"(hw), "+v"(bact), "+v"(a128), "+v"(syn), "+v"(o_qs), "+v"(one), "+v"(T4.m), "+v"(sa));
  asm volatile("s_waitcnt lgkmcnt(0)" ::"s"(pf0), "s"(pf1), "s"(pf2));
  await<C_X, NLW>(syn);
  ST(1);
  {
    const v4u xa = *(LAS v4u *)xa_, xb = *(LAS v4u *)(xa_ + 1024);
    const v2u xo = *(LAS v2u *)xo_;
    f32x2 v[8];
#pragma unroll
    for (int i = 0; i < 4; ++i) {
      v[i] = f32x2{blo(xa[i]), bhi(xa[i])};
      v[4 + i] = f32x2{blo(xb[i]), bhi(xb[i])};
    }
    const f32x2 s = ((v[0] + v[1]) + (v[2] + v[3])) + ((v[4] + v[5]) + (v[6] + v[7]));
    const float mean = sum64(s.x + s.y) * (1.f / 1024.f);
    f32x2 qq = {0.f, 0.f};
#pragma unroll
    for (int i = 0; i < 8; ++i) qq += (v[i] - mean) * (v[i] - mean);
    const float var = rbf(sum64(qq.x + qq.y) * (1.f / 1024.f));
    const float m = rbf(mean), ri = __builtin_amdgcn_rcpf(rbf(__builtin_amdgcn_sqrtf(rbf(var + 1e-5f))));
    const unsigned d0 = pkbf(blo(xo.x) - m, bhi(xo.x) - m), d1 = pkbf(blo(xo.y) - m, bhi(xo.y) - m);
    *(LAS v2u *)hw = v2u{pkbf(blo(d0) * ri, bhi(d0) * ri), pkbf(blo(d1) * ri, bhi(d1) * ri)};
  }
  arrive<C_XN>(sa, one);
  ST(2);
  unsigned Ev;
  if (w < 4) {
    await<C_XN, NCW>(syn);
    v8s af[8];
    v4u bf[8];
    rd_b<8>(bact, bf);
    await<C_Q, NLW>(syn);
    Ev = *(LAS unsigned *)(syn + (L_EP - L_SYNC));
    rd_a<8, 4096>(a128, af);
    SCHED();
    const unsigned E0 = __builtin_amdgcn_readfirstlane(Ev);
    T4.t0 = E0 + 1u, T4.t1 = (E0 >> 8) + 1u, T4.t2 = (E0 >> 16) + 1u, T4.t3 = (E0 >> 24) + 1u;
    stws(rs, o_qs, tag4(mm<8>(af, bf), T4));
  } else {
    await<C_Q, NLW, 2>(syn);
    Ev = *(LAS unsigned *)(syn + (L_EP - L_SYNC));
  }
  ST(3);
  const unsigned E = __builtin_amdgcn_readfirstlane(Ev);
  const unsigned Tw = ((E & 0x7f7f7f7fu) + 0x01010101u) ^ (E & 0x80808080u);
  T.t0 = Tw & 0xffu, T.t1 = (Tw >> 8) & 0xffu, T.t2 = (Tw >> 16) & 0xffu, T.t3 = Tw >> 24;
  T4.t0 = T.t0 & 0xfu, T4.t1 = T.t1 & 0xfu, T4.t2 = T.t2 & 0xfu, T4.t3 = T.t3 & 0xfu;
  SCHED();
  const int rr = xcd * 4 + kq, rb = 2 * (rr >> 3) + ((l >> 2) & 1), rcol = (rr & 7) * 128 + 32 * (w & 3) + 16 * ((l >> 3) & 1) + 4 * p4;
  const int dcu = xcd * 28 + j, eu = (w == 0) ? dcu : 224 + dcu;
  const bool hasE = (w == 0) || (w == 1 && dcu < 32);
  const int drow = 16 * ((trow >> 1) & 3) + 2 * (g >> 1) + (trow & 1);
  LAS unsigned char *a256 = L + L_R2 + (32 * g + q4) * 256 + (p4 & 1) * 8 + (((2 * w + (p4 >> 1)) ^ sw256) * 16);
  LAS unsigned char *kp[4];
#pragma unroll
  for (int i = 0; i < 4; ++i) kp[i] = L + L_R1 + (32 * q4 + 4 * w + p4) * 256 + (((4 * i + g) ^ col) * 16);
  LAS unsigned char *qw = L + L_ACT2 + ((l >> 3) & 3) * 512 + ((l >> 1) & 3) * 128 + w * 16 + (l & 1) * 8 + (l >> 5) * 4;
  LAS unsigned char *mxw = L + (l < 8 ? L_MX + l * 32 + w * 4 : L_JUNK + 256 + 4 * l), *mxr = L + L_MX + b8 * 32;
  LAS unsigned char *pw = col < 8 ? bact + (w >> 1) * 512 + (w & 1) * 8 : L + L_JUNK + 1024 + 8 * l;
  LAS unsigned char *ow = L + L_ACT2 + ((l >> 3) & 3) * 512 + p4 * 128 + w * 16 + ((l >> 2) & 1) * 8 + (l >> 5) * 4;
  LAS unsigned char *dw = L + L_ACT2 + (w & 1) * 512 + p4 * 128 + (2 * (w >> 1) + ((l >> 2) & 1)) * 16 + (col >> 3) * 8 + g * 2;
  LAS unsigned char *ad0 = L + L_R2 + drow * 512 + (p4 & 1) * 8 + (((4 * w + (p4 >> 1)) ^ sw256) * 16);
  LAS unsigned char *ad1 = L + L_R2 + drow * 512 + (p4 & 1) * 8 + (((4 * w + 2 + (p4 >> 1)) ^ sw256) * 16);
  int o_qg = WS_QP + ((akv * 4 + (ab >> 1)) * 8 + w) * 4096 + (l >> 2) * 128 + (ab & 1) * 64 + p4 * 16;
  int o_as = WS_AP + (((akv * 8 + (b8 >> 1) * 2 + (w >> 2)) * 64 + ab * 8 + as) * 4 + (w & 3)) * 128 + lst + dead;
  int o_ms = WS_ML + (akv * 64 + ab * 8 + as) * 128 + l * 16 + (l < 8 ? 0 : OOB);
  int o_mg = WS_ML + ((ku >> 3) * 64 + w * 8 + 4 * (l >> 5) + p4) * 128 + (2 * ((ku >> 1) & 3) + ((l >> 2) & 1)) * 16;
  int o_ag = WS_AP + (ku * 64 + w * 8 + 4 * (l >> 5)) * 512 + (l & 31) * 16;
  int o_os = WS_OP + ((((b8 >> 1) * 32 + ocg * 4 + (w >> 1)) * 32 + ku) * 2 + (w & 1)) * 128 + lst + dead;
  int o_s2 = isG ? WS_GP + ((fg * 4 + kq) * 8 + w) * 512 + b8 * 64 + g * 16 + dead : WS_HB + (rb * 1024 + rcol) * 4 + (l < 16 && w < 4 ? 0 : OOB);
  int o_g2 = isG ? WS_HB + w * 4096 + l * 16 : WS_OP + (rr * 4 + w) * 8192 + l * 16;
  int o_ds = isG ? WS_DP + ((b8 * 32 + 8 * kq + w) * 56 + fg) * 128 + (col >> 3) * 64 + g * 16 : OOB;
  int o_gg = WS_GP + ((fg * 4 + g) * 8 + 2 * (w & 1) + (col >> 3)) * 512 + (w >> 1) * 128 + b8 * 16;
  int o_eh = WS_HB + eu * 128 + b8 * 16, o_dg = WS_DP + eu * 7168 + l * 16, o_out = (eu * 32 + 4 * b8 + g) * 2 + dead;
  v2u xr = *(LAS v2u *)(L + L_X + (rb * 1024 + rcol) * 2);
  asm volatile("" : "+v"(xr), "+v"(a256), "+v"(kp[0]), "+v"(kp[1]), "+v"(kp[2]), "+v"(kp[3]), "+v"(qw), "+v"(mxw), "+v"(mxr), "+v"(pw), "+v"(ow),
               "+v"(dw), "+v"(ad0), "+v"(ad1));
  asm volatile("" : "+v"(o_qg), "+v"(o_as), "+v"(o_ms), "+v"(o_mg), "+v"(o_ag), "+v"(o_os), "+v"(o_s2), "+v"(o_g2), "+v"(o_ds), "+v"(o_gg),
               "+v"(o_eh), "+v"(o_dg), "+v"(o_out), "+v"(T.m));
  const v4u bad = {~T.t0, ~T.t1, ~T.t2, ~T.t3};
  stws(rs, o_as, bad);
  await<C_K, NLW, 1>(syn);
  __builtin_amdgcn_s_sleep(DLY_Q);
  float M;
  v4u pf[4];
  {
    v8s kf[4];
#pragma unroll
    for (int i = 0; i < 4; ++i) kf[i] = *(LAS v8s *)kp[i];
    f32x4 a[2];
    gather<2, 2048, 0xfu>(rs, o_qg, a, T4);
    ST(4);
    const f32x4 s = a[0] + a[1];
    *(LAS unsigned *)qw = pkbf(rs32(s.x, s.z), rs32(s.y, s.w));
    arrive<C_QL>(sa, one);
    await<C_QL, NCW>(syn);
    ST(5);
    v4u qf[4];
    rd_b<4>(bact + 4096, qf);
    const f32x4 acc = mm<4>(kf, qf);
    const unsigned r0 = pkbf(acc.x, acc.y), r1 = pkbf(acc.z, acc.w);
    const float SCL = 0.08838834764831845f;
    const unsigned s0 = pkbf(blo(r0) * SCL, bhi(r0) * SCL), s1 = pkbf(blo(r1) * SCL, bhi(r1) * SCL);
    const float sc0 = blo(s0), sc1 = bhi(s0), sc2 = blo(s1), sc3 = bhi(s1);
    *(LAS float *)mxw = mx32(mx16(fmx(fmx(sc0, sc1), fmx(sc2, sc3))));
    arrive<C_MX>(sa, one);
    await<C_MX, NCW>(syn);
    const f32x4 m0 = *(LAS f32x4 *)mxr, m1 = *(LAS f32x4 *)(mxr + 16);
    M = fmx(fmx(fmx(m0.x, m0.y), fmx(m0.z, m0.w)), fmx(fmx(m1.x, m1.y), fmx(m1.z, m1.w)));
    const float p0 = __builtin_amdgcn_exp2f((sc0 - M) * LOG2E), p1 = __builtin_amdgcn_exp2f((sc1 - M) * LOG2E),
                p2 = __builtin_amdgcn_exp2f((sc2 - M) * LOG2E), p3 = __builtin_amdgcn_exp2f((sc3 - M) * LOG2E);
    *(LAS v2u *)pw = v2u{pkbf(p0, p1), pkbf(p2, p3)};
    float ls = (p0 + p1) + (p2 + p3);
    ls = rs16(ls, ls);
    *(LAS float *)(mxw + 256) = rs32(ls, ls);
    arrive<C_P>(sa, one);
    await<C_P, NCW>(syn);
    rd_b<4>(bact, pf);
    if (w == 7) {
      const f32x4 l2 = *(LAS f32x4 *)(mxr + 256) + *(LAS f32x4 *)(mxr + 272);
      stws(rs, o_ms, tag4(f32x4{M, (l2.x + l2.y) + (l2.z + l2.w), 0.f, 0.f}, T));
    }
  }
  ST(6);
  await<C_V, NLW>(syn);
  {
    v8s af[4];
    rd_a<4, 2048>(a256, af);
    stws(rs, o_as, tag4(mm<4>(af, pf), T));
  }
  ST(7);
  stws(rs, o_os, bad);
  stws(rs, o_s2, bad);
  stws(rs, o_ds, bad);
  {
    f32x4 ml[1];
    gather<1, 0, 0xffu, true>(rs, o_mg, ml, T);
    float mm_ = fmx(ml[0].x, dpp<XOR1>(ml[0].x));
    mm_ = mx32(fmx(mm_, dpp<XOR2>(mm_)));
    const float wt = __builtin_amdgcn_exp2f((ml[0].x - mm_) * LOG2E);
    float ll = wt * ml[0].y;
    ll += dpp<XOR1>(ll);
    ll += dpp<XOR2>(ll);
    ll = rs32(ll, ll);
    const float cf = wt * __builtin_amdgcn_rcpf(ll);
    float c0 = dpp<0x00>(cf), c1 = dpp<0x55>(cf), c2 = dpp<0xAA>(cf), c3 = dpp<0xFF>(cf);
    asm volatile("" : "+v"(c0), "+v"(c1), "+v"(c2), "+v"(c3));
    await<C_WO, NLW>(syn);
    ST(8);
    v8s af[4];
    rd_a<4, 2048>(a256 + 32768, af);
    f32x4 a[4];
    gather<4, 512>(rs, o_ag, a, T);
    ST(9);
    const f32x4 o = (a[0] * c0 + a[1] * c1) + (a[2] * c2 + a[3] * c3);
    *(LAS unsigned *)ow = pkbf(rs32(o.x, o.z), rs32(o.y, o.w));
    BAR();
    v4u bf[4];
    rd_b<4>(bact + 4096, bf);
    stws(rs, o_os, tag4(mm<4>(af, bf), T));
  }
  ST(10);
  if (!isG) {
    if (w < 4) {
      __builtin_amdgcn_s_sleep(DLY_R);
      f32x4 a[8];
      gather<8, 1024>(rs, o_g2, a, T);
      ST(11);
      f32x4 s = ((a[0] + a[1]) + (a[2] + a[3])) + ((a[4] + a[5]) + (a[6] + a[7]));
#pragma unroll
      for (int i = 0; i < 4; ++i) {
        const float t = rs16(s[i], s[i]);
        s[i] = rs32(t, t);
      }
      const unsigned t0 = pkbf(s.x, s.y), t1 = pkbf(s.z, s.w);
      const unsigned h0 = pkbf(blo(xr.x) + blo(t0), bhi(xr.x) + bhi(t0)), h1 = pkbf(blo(xr.y) + blo(t1), bhi(xr.y) + bhi(t1));
      stws(rs, o_s2, v4u{(h0 << 16) | T.t0, (h0 & 0xffff0000u) | T.t1, (h1 << 16) | T.t2, (h1 & 0xffff0000u) | T.t3});
      ST(12);
    }
  } else {
    await<C_H, NLW>(syn);
    {
      v8s af[8];
      rd_a<8, 4096>(a128, af);
      f32x4 h[4];
      gather<4, 1024>(rs, o_g2, h, T);
      ST(11);
      const f32x4 sv = (h[0] + h[1]) + (h[2] + h[3]);
      const float mean = sum64((sv.x + sv.y) + (sv.z + sv.w)) * (1.f / 1024.f);
      const f32x4 d0 = h[0] - mean, d1 = h[1] - mean, d2 = h[2] - mean, d3 = h[3] - mean;
      const f32x4 qv = (d0 * d0 + d1 * d1) + (d2 * d2 + d3 * d3);
      const float var = rbf(sum64((qv.x + qv.y) + (qv.z + qv.w)) * (1.f / 1024.f));
      const float m = rbf(mean), ri = __builtin_amdgcn_rcpf(rbf(__builtin_amdgcn_sqrtf(rbf(var + 1e-5f))));
      const f32x4 ho = (kq & 2) ? ((kq & 1) ? h[3] : h[2]) : ((kq & 1) ? h[1] : h[0]);
      const unsigned e0 = pkbf(ho.x - m, ho.y - m), e1 = pkbf(ho.z - m, ho.w - m);
      *(LAS v2u *)hw = v2u{pkbf(blo(e0) * ri, bhi(e0) * ri), pkbf(blo(e1) * ri, bhi(e1) * ri)};
      BAR();
      ST(12);
      v4u bf[8];
      rd_b<8>(bact, bf);
      stws(rs, o_s2, tag4(mm<8>(af, bf), T));
    }
    if (c == 0 && w == 2 && l == 0) __hip_atomic_store((GAS unsigned *)ws, Tw, __ATOMIC_RELAXED, __HIP_MEMORY_SCOPE_AGENT);
    float hv = 0.f;
    if (hasE) {
      f32x4 hE[1];
      gather<1, 0>(rs, o_eh, hE, T);
      hv = (g & 2) ? ((g & 1) ? hE[0].w : hE[0].z) : ((g & 1) ? hE[0].y : hE[0].x);
    }
    __builtin_amdgcn_s_sleep(DLY_D);
    {
      v8s a0[2], a1[2];
      rd_a<2, 4096>(ad0, a0);
      rd_a<2, 4096>(ad1, a1);
      f32x4 a[2];
      gather<2, 2048>(rs, o_gg, a, T);
      ST(13);
      const unsigned gu = pkbf(rs16(rs32(a[0].x, a[0].z), rs32(a[0].y, a[0].w)), rs16(rs32(a[1].x, a[1].z), rs32(a[1].y, a[1].w)));
      const float sg = rbf(__builtin_amdgcn_rcpf(1.f + __builtin_amdgcn_exp2f(blo(gu) * -LOG2E)));
      *(LAS u16 *)dw = (u16)pkbf(sg * bhi(gu), 0.f);
      BAR();
      v4u bf[2];
      rd_b<2>(bact + 4096, bf);
      SCHED();
      f32x4 c0 = {0.f, 0.f, 0.f, 0.f}, c1 = {0.f, 0.f, 0.f, 0.f};
      c0 = mfma(a0[0], bf[0], c0);
      c1 = mfma(a1[0], bf[0], c1);
      c0 = mfma(a0[1], bf[1], c0);
      c1 = mfma(a1[1], bf[1], c1);
      stws(rs, o_ds, tag4(col < 8 ? c0 : c1, T));
    }
    ST(14);
    if (hasE) {
      __builtin_amdgcn_s_sleep(DLY_E);
      f32x4 a[7];
      gather<7, 1024>(rs, o_dg, a, T);
      const f32x4 s = ((a[0] + a[1]) + (a[2] + a[3])) + ((a[4] + a[5]) + a[6]);
      float t = rs16(rs32(s.x, s.z), rs32(s.y, s.w));
      t += dpp<XOR8>(t);
      __builtin_amdgcn_raw_buffer_store_b16((u16)pkbf(hv + rbf(t), 0.f), mkrs(out, 8 * 1024 * 2), o_out, 0, 0);
    }
  }
  STOUT();
}

#define TSTRIDE 1048576
__global__ void pre_kernel(const unsigned char *x, const unsigned char *wq, const unsigned char *kc, const unsigned char *vc, const unsigned char *wo,
    const unsigned char *wg, const unsigned char *wu, const unsigned char *wd, const unsigned char *out, unsigned char *ws) {
  const int j = blockIdx.x >> 3, t = j * 256 + threadIdx.x;
  unsigned acc = 0;
  const unsigned char *p[10] = {x, wq, kc, vc, wo, wg, wu, wd, out, ws};
  const int sz[10] = {16384, 8 << 20, 32 << 20, 32 << 20, 8 << 20, 7 << 20, 7 << 20, 7 << 20, 16384, 6 << 20};
#pragma unroll
  for (int b = 0; b < 10; ++b)
    for (long o = (long)t * TSTRIDE; o < sz[b]; o += (long)8192 * TSTRIDE)
      if ((b != 2 && b != 3) || TSTRIDE >= (1 << 20) || ((o >> 18) & 3) == 3) acc += __builtin_nontemporal_load((const unsigned *)(p[b] + o));
  if (acc == 0x12345678u) ((unsigned *)ws)[1 << 22] = acc;
}
extern "C" void kernel_launch(void *const *d_in, const int *in_sizes, int n_in, void *d_out, int out_size, void *d_ws, size_t ws_size,
    hipStream_t stream) {
  (void)in_sizes, (void)n_in, (void)out_size, (void)ws_size;
  (void)hipFuncSetAttribute((const void *)blk_kernel, hipFuncAttributeMaxDynamicSharedMemorySize, L_END);
  pre_kernel<<<256, 256, 0, stream>>>((const unsigned char *)d_in[0], (const unsigned char *)d_in[3], (const unsigned char *)d_in[1], (const unsigned char *)d_in[2],
      (const unsigned char *)d_in[4], (const unsigned char *)d_in[6], (const unsigned char *)d_in[7], (const unsigned char *)d_in[8], (const unsigned char *)d_out, (unsigned char *)d_ws);
  blk_kernel<<<256, NTHR, L_END, stream>>>((const u16 *)d_in[0], (const u16 *)d_in[3], (const u16 *)d_in[1], (const u16 *)d_in[2],
      (const u16 *)d_in[4], (const u16 *)d_in[6], (const u16 *)d_in[7], (const u16 *)d_in[8], (u16 *)d_out, (unsigned char *)d_ws);
}
```

```cpp
#define PROBE 1
#define PROBEW 4
#define PROBEADD 0
#include <hip/hip_runtime.h>

#define GAS __attribute__((address_space(1)))
#define LAS __attribute__((address_space(3)))
typedef unsigned short u16;
typedef unsigned long long u64;
typedef unsigned v4u __attribute__((ext_vector_type(4)));
typedef unsigned v2u __attribute__((ext_vector_type(2)));
typedef float f32x4 __attribute__((ext_vector_type(4)));
typedef short v4s __attribute__((ext_vector_type(4)));
typedef short v8s __attribute__((ext_vector_type(8)));
typedef __bf16 v8bf __attribute__((ext_vector_type(8)));
typedef __bf16 v2bf __attribute__((ext_vector_type(2)));
typedef float f32x2 __attribute__((ext_vector_type(2)));

#define AUXW 2
#define WIN 6
#define EPOFF 0
#define WIN2 8
#define DLY_Q 0
#define DLY_R 12
#define DLY_D 8
#define DLY_E 14
#define H_TAIL 3
#define WG_EARLY 0
#define PREW 3
#define WARM 1
#define CODE_BYTES 10496

namespace {
constexpr int NCW = 8, NLW = 4, NTHR = (NCW + NLW) * 64;
constexpr int WS_EPOCH = 0;
constexpr int WS_QP = 4096;
constexpr int WS_AP = WS_QP + 4 * 8 * 4096 * 4;
constexpr int WS_ML = WS_AP + 32 * 64 * 128 * 4;
constexpr int WS_OP = WS_ML + 32 * 64 * 16;
constexpr int WS_HB = WS_OP + 32 * 8 * 1024 * 4;
constexpr int WS_GP = WS_HB + 8 * 1024 * 4;
constexpr int WS_DP = WS_GP + 56 * 16384;
constexpr int WS_END = WS_DP + 56 * 8 * 1024 * 4;
constexpr int WS_STAMP = (WS_END + 4095) / 4096 * 4096;
constexpr int OOB = 0x40000000;
constexpr int L_R0 = 0, L_R1 = 32768, L_R2 = 65536, L_R3 = 98304, L_X = 131072, L_ACT = L_X + 16384, L_ACT2 = L_ACT + 4096,
              L_MX = L_ACT2 + 2048, L_LS = L_MX + 256, L_SYNC = L_LS + 256, L_EP = L_SYNC + 64, L_JUNK = L_EP + 256, L_END = L_JUNK + 4096;
enum { C_X = 0, C_XN, C_Q, C_K, C_QL, C_MX, C_P, C_V, C_WO, C_H, C_N };
constexpr int SC1 = 16;
constexpr unsigned SPIN_CAP = 1u << 16;
constexpr float LOG2E = 1.4426950408889634f;

#define BAR() asm volatile("s_waitcnt lgkmcnt(0)\n\ts_barrier" ::: "memory")
#define VMWAIT(n) asm volatile("s_waitcnt vmcnt(%0)" ::"n"(n) : "memory")
#define SCHED() __builtin_amdgcn_sched_barrier(0)
#define PIN(x) asm volatile("" : "+v"(x))

__device__ __forceinline__ float asf(unsigned u) { return __builtin_bit_cast(float, u); }
__device__ __forceinline__ unsigned asu(float f) { return __builtin_bit_cast(unsigned, f); }
__device__ __forceinline__ unsigned pkbf(float lo, float hi) {
  f32x2 v = {lo, hi};
  return __builtin_bit_cast(unsigned, __builtin_convertvector(v, v2bf));
}
__device__ __forceinline__ float blo(unsigned p) { return asf(p << 16); }
__device__ __forceinline__ float bhi(unsigned p) { return asf(p & 0xffff0000u); }
__device__ __forceinline__ float rbf(float f) { return blo(pkbf(f, f)); }
__device__ __forceinline__ float fmx(float a, float b) { return __builtin_fmaxf(a, b); }
template <int CTRL> __device__ __forceinline__ float dpp(float x) {
  return asf(__builtin_amdgcn_mov_dpp(asu(x), CTRL, 0xf, 0xf, true));
}
constexpr int XOR1 = 0xB1, XOR2 = 0x4E, XOR7 = 0x141, XOR8 = 0x128;
__device__ __forceinline__ float rs32(float a, float b) {
  auto r = __builtin_amdgcn_permlane32_swap(asu(a), asu(b), false, false);
  return asf(r[0]) + asf(r[1]);
}
__device__ __forceinline__ float rs16(float a, float b) {
  auto r = __builtin_amdgcn_permlane16_swap(asu(a), asu(b), false, false);
  return asf(r[0]) + asf(r[1]);
}
__device__ __forceinline__ float mx32(float a) {
  auto r = __builtin_amdgcn_permlane32_swap(asu(a), asu(a), false, false);
  return fmx(asf(r[0]), asf(r[1]));
}
__device__ __forceinline__ float mx16(float a) {
  auto r = __builtin_amdgcn_permlane16_swap(asu(a), asu(a), false, false);
  return fmx(asf(r[0]), asf(r[1]));
}
__device__ __forceinline__ float sum64(float x) {
  x += dpp<XOR1>(x);
  x += dpp<XOR2>(x);
  x += dpp<XOR7>(x);
  x += dpp<XOR8>(x);
  x = rs16(x, x);
  return rs32(x, x);
}
struct Tag {
  unsigned t0, t1, t2, t3;
  unsigned m;
};
__device__ __forceinline__ v4u tag4(f32x4 v, const Tag &T) {
  v4u o;
  o.x = (T.m & T.t0) | (~T.m & asu(v.x));
  o.y = (T.m & T.t1) | (~T.m & asu(v.y));
  o.z = (T.m & T.t2) | (~T.m & asu(v.z));
  o.w = (T.m & T.t3) | (~T.m & asu(v.w));
  return o;
}
__device__ __forceinline__ v4u ldws(__amdgpu_buffer_rsrc_t rs, int off) { return __builtin_amdgcn_raw_buffer_load_b128(rs, off, 0, SC1); }
__device__ __forceinline__ void stws(__amdgpu_buffer_rsrc_t rs, int off, v4u v) { __builtin_amdgcn_raw_buffer_store_b128(v, rs, off, 0, SC1); }
__device__ __forceinline__ f32x4 mfma(v8s a, v4u b, f32x4 c) {
  return __builtin_amdgcn_mfma_f32_16x16x32_bf16(__builtin_bit_cast(v8bf, a), __builtin_bit_cast(v8bf, b), c, 0, 0, 0);
}
__device__ __forceinline__ v8s trfrag(LAS unsigned char *p, int off0, int off1) {
  const v4s a = __builtin_amdgcn_ds_read_tr16_b64_v4i16((LAS v4s *)(p + off0));
  const v4s b = __builtin_amdgcn_ds_read_tr16_b64_v4i16((LAS v4s *)(p + off1));
  return __builtin_shufflevector(a, b, 0, 1, 2, 3, 4, 5, 6, 7);
}
template <int NK, int KS> __device__ __forceinline__ void rd_a(LAS unsigned char *a, v8s (&af)[NK]) {
#pragma unroll
  for (int ks = 0; ks < NK; ++ks) af[ks] = trfrag(a, ks * KS, ks * KS + KS / 2);
}
template <int NK> __device__ __forceinline__ void rd_b(LAS unsigned char *b, v4u (&bf)[NK]) {
#pragma unroll
  for (int ks = 0; ks < NK; ++ks) bf[ks] = *(LAS v4u *)(b + ks * 512);
}
template <int NK> __device__ __forceinline__ f32x4 mm(const v8s (&af)[NK], const v4u (&bf)[NK]) {
  SCHED();
  f32x4 acc = {0.f, 0.f, 0.f, 0.f};
#pragma unroll
  for (int ks = 0; ks < NK; ++ks) acc = mfma(af[ks], bf[ks], acc);
  return acc;
}
template <int NK> __device__ __forceinline__ void pin_a(v8s (&af)[NK]) {
#pragma unroll
  for (int ks = 0; ks < NK; ++ks) PIN(af[ks]);
}
template <int N, int STEP, unsigned MASK = 0xffu, bool ZW = false>
__device__ __forceinline__ void gather(__amdgpu_buffer_rsrc_t rs, int off, f32x4 (&v)[N], const Tag &T) {
  unsigned spins = 0;
#pragma unroll 1
  for (;;) {
    asm volatile("" ::: "memory");
    v4u a[N];
#pragma unroll
    for (int i = 0; i < N; ++i) a[i] = ldws(rs, off + i * STEP);
    unsigned d = 0;
#pragma unroll
    for (int i = 0; i < N; ++i) {
      a[i].x ^= T.t0, a[i].y ^= T.t1, a[i].z ^= T.t2, a[i].w ^= T.t3;
      d |= (a[i].x | a[i].y) | (a[i].z | a[i].w);
      v[i] = __builtin_bit_cast(f32x4, a[i]);
    }
    d &= MASK;
    if (ZW) d |= a[0].z | a[0].w;
    if (__builtin_expect(__builtin_amdgcn_ballot_w64(d != 0u) == 0ull, 1)) break;
    if (++spins > SPIN_CAP) break;
  }
}
__device__ __forceinline__ __amdgpu_buffer_rsrc_t mkrs(const void *p, int bytes) {
  return __builtin_amdgcn_make_buffer_rsrc((void *)p, 0, bytes, 0x00020000);
}
template <int AUX, int IMM = 0> __device__ __forceinline__ void dma1(__amdgpu_buffer_rsrc_t rs, LAS unsigned char *dst, int voff, int soff) {
  __builtin_amdgcn_raw_ptr_buffer_load_lds(rs, (LAS void *)dst, 16, voff, soff, IMM, AUX);
}
template <int W> __device__ __forceinline__ void dmal(__amdgpu_buffer_rsrc_t rs, LAS unsigned char *&dst, int voff, int &soff, int sstep, int n) {
#pragma unroll 1
  for (int i = 0; i < n; ++i) {
    if (W < 64) VMWAIT(W - 1);
    dma1<AUXW>(rs, dst, voff, soff);
    dst += 1024, soff += sstep;
  }
}
template <int K> __device__ __forceinline__ void arrive(int sa, unsigned one = 1u) {
  asm volatile("ds_add_u32 %0, %1 offset:%2" ::"v"(sa), "v"(one), "n"(4 * K) : "memory");
}
template <int K, int N, int SLEEP = 0> __device__ __forceinline__ void await(LAS unsigned char *syn) {
  while (__builtin_amdgcn_readfirstlane(*(volatile LAS unsigned *)(syn + 4 * K)) < (unsigned)N) {
    if (SLEEP) __builtin_amdgcn_s_sleep(SLEEP);
  }
  asm volatile("" ::: "memory");
}
#define ST(i) do { if ((i) == PROBE) stp = (unsigned)__builtin_amdgcn_s_memrealtime(); } while (0)
#define STOUT() do { if (wv == PROBEW) { const unsigned e_ = (unsigned)__builtin_amdgcn_s_memrealtime() + (stp - st0) + PROBEADD; \
  while ((int)((unsigned)__builtin_amdgcn_s_memrealtime() - e_) < 0) __builtin_amdgcn_s_sleep(2); } } while (0)
}

__global__ void __launch_bounds__(NTHR, 3)
    blk_kernel(const u16 *x, const u16 *wq, const u16 *kc, const u16 *vc, const u16 *wo, const u16 *wg, const u16 *wu, const u16 *wd, u16 *out,
        unsigned char *ws) {
  extern __shared__ __attribute__((aligned(16))) unsigned char smem_[];
  LAS unsigned char *L = (LAS unsigned char *)smem_;
  u64 pc;
  asm volatile("s_getpc_b64 %0" : "=s"(pc));
  const int tid = threadIdx.x, l = tid & 63, wv = __builtin_amdgcn_readfirstlane(tid >> 6);
  const int c = blockIdx.x, xcd = c & 7, j = c >> 3;
  const int kq = j & 3;
  const int cg64 = (j >> 2) * 8 + xcd;
  const int ab = xcd, akv = j >> 3, as = j & 7;
  const int ku = 4 * xcd + (j >> 3), ocg = j & 7;
  const bool isG = j < 28;
  const int fg = xcd * 7 + (j >> 2);
  int sa = l == 0 ? L_SYNC : L_JUNK + 4 * l;
  const unsigned st0 = (unsigned)__builtin_amdgcn_s_memrealtime();
  unsigned stp = st0;

  const int lw = wv & 3;
  asm volatile("ds_write_b32 %0, %1" ::"v"(L_SYNC + 4 * (l & 15)), "v"(0u) : "memory");
  const int v128 = ((l & 7) ^ (((l >> 4) & 3) << 1)) * 16;
  int vx = l * 16, vq = (l >> 3) * 8192 + v128;
  int so = (kq * 256 + lw * 64) * 8192 + cg64 * 128, sx = lw * 4096;
  LAS unsigned char *d = L + L_R0 + lw * 8192, *dx = L + L_X + lw * 4096;
  asm volatile("" : "+v"(vx), "+v"(vq), "+s"(so), "+s"(sx), "+s"(d), "+s"(dx));
  asm volatile("s_waitcnt lgkmcnt(0)\n\ts_barrier" ::"s"(x), "s"(wq), "s"(kc), "s"(vc), "s"(wo), "s"(wg), "s"(wu), "s"(wd), "s"(out), "s"(ws)
               : "memory");
  if (__builtin_expect(wv < NLW, 1)) {
    {
      const __amdgpu_buffer_rsrc_t rx = mkrs(x, 8 * 1024 * 2);
      dma1<0, 0>(rx, dx, vx, sx);
      dma1<0, 1024>(rx, dx, vx, sx);
      dma1<0, 2048>(rx, dx, vx, sx);
      dma1<0, 3072>(rx, dx, vx, sx);
    }
    dmal<64>(mkrs(wq, 1024 * 4096 * 2), d, vq, so, 8 * 8192, 8);
    __builtin_amdgcn_raw_ptr_buffer_load_lds(mkrs(ws, 4096), (LAS void *)(L + L_EP), 4, 0, 0, 0, SC1);
    ST(1);
    VMWAIT(9);
    arrive<C_X>(sa);
    ST(2);
    const int kvbase = ((ab * 4 + akv) * 4096 + 3072 + as * 128 + lw * 32) * 256;
    const int r4 = l >> 4;
    const int v256 = ((l & 15) ^ ((4 * (lw & 1) + r4) << 1)) * 16;
    {
      const __amdgpu_buffer_rsrc_t rk = mkrs(kc, 8 * 4 * 4096 * 128 * 2);
      const int vk = r4 * 256 + (((l & 15) ^ (4 * lw + r4)) * 16);
      d += 32768 - 8192, so = kvbase;
      dmal<WIN>(rk, d, vk, so, 1024, WIN);
      arrive<C_Q>(sa);
      ST(3);
      dmal<WIN>(rk, d, vk, so, 1024, 8 - WIN);
    }
    {
      const __amdgpu_buffer_rsrc_t rv = mkrs(vc, 8 * 4 * 4096 * 128 * 2);
      d += 32768 - 8192, so = kvbase;
      dmal<WIN>(rv, d, r4 * 256 + v256, so, 1024, WIN);
      arrive<C_K>(sa);
      ST(4);
      dmal<WIN>(rv, d, r4 * 256 + v256, so, 1024, 8 - WIN);
    }
    {
      const __amdgpu_buffer_rsrc_t ro = mkrs(wo, 4096 * 1024 * 2);
      so = ((ku >> 1) * 256 + (ku & 1) * 64 + 4 * lw) * 2048 + ocg * 256;
      d += 32768 - 8192;
#pragma unroll 1
      for (int i = 0; i < 8; ++i) {
        VMWAIT(WIN - 1);
        if (i == WIN - 1) arrive<C_V>(sa);
        dma1<AUXW>(ro, d + i * 1024, r4 * 2048 + v256, so + ((i & 1) * 128 + (i >> 1) * 16) * 2048);
      }
    }
    ST(5);
    VMWAIT(0);
    arrive<C_WO>(sa);
    ST(6);
    BAR();
    ST(7);
    if (!isG) {
      STOUT();
      return;
    }
    const int sg = (kq * 256 + lw * 64) * 7168 + fg * 128;
    d = L + L_R0 + lw * 8192, so = sg;
    dmal<WIN2>(mkrs(wg, 1024 * 3584 * 2), d, (l >> 3) * 7168 + v128, so, 8 * 7168, 8);
    d += 32768 - 8192, so = sg;
    dmal<WIN2>(mkrs(wu, 1024 * 3584 * 2), d, (l >> 3) * 7168 + v128, so, 8 * 7168, 8);
    ST(8);
    {
      d += 32768 - 8192, so = (fg * 64 + 2 * lw) * 2048 + kq * 512;
      dmal<WIN2>(mkrs(wd, 3584 * 1024 * 2), d, (l >> 5) * 2048 + (((l & 31) ^ ((2 * lw + (l >> 5)) << 1)) * 16), so, 8 * 2048, 8);
    }
    ST(9);
    VMWAIT(H_TAIL);
    arrive<C_H>(sa);
    ST(10);
    VMWAIT(0);
    ST(11);
    STOUT();
    return;
  }

  const int w = wv - NLW;
  unsigned pf0, pf1, pf2;
  {
    const u64 cb = pc & ~255ull;
    const int o0 = (w + 24 * kq) * 128;
    asm volatile("s_load_dword %0, %3, %4\n\ts_load_dword %1, %3, %5\n\ts_load_dword %2, %3, %6"
                 : "=&s"(pf0), "=&s"(pf1), "=&s"(pf2)
                 : "s"(cb), "s"(min(o0, CODE_BYTES - 4)), "s"(min(o0 + 1024, CODE_BYTES - 4)), "s"(min(o0 + 2048, CODE_BYTES - 4)));
  }
  const int g = l >> 4, col = l & 15, b8 = l & 7, q4 = (l >> 2) & 3, p4 = l & 3;
  const int lst = (l & 1) * 64 + g * 16;
  const int dead = col < 8 ? 0 : OOB;
  const int trow = 4 * g + q4;
  const int sw128 = ((trow >> 1) & 3) << 1, sw256 = (trow & 7) << 1;
  LAS unsigned char *hw = L + L_ACT + (l >> 3) * 512 + (l & 3) * 128 + w * 16 + ((l >> 2) & 1) * 8;
  LAS unsigned char *bact = L + L_ACT + g * 128 + b8 * 16;
  LAS unsigned char *a128 = L + trow * 128 + (p4 & 1) * 8 + (((2 * (w & 3) + (p4 >> 1)) ^ sw128) * 16) + (w >> 2) * 32768;
  LAS unsigned char *syn = L + L_SYNC;
  int o_qs = WS_QP + (((((cg64 >> 4) * 4 + (b8 >> 1)) * 8 + ((cg64 >> 1) & 7)) * 4 + kq) * 8 + (cg64 & 1) * 4 + w) * 128 + lst + dead;
  LAS unsigned char *xa_ = L + L_X + w * 2048 + l * 16, *xo_ = L + L_X + w * 2048 + kq * 512 + l * 8;
  unsigned one = 1u;
  Tag T, T4;
  T.m = 0xffu, T4.m = 0xfu;
  const __amdgpu_buffer_rsrc_t rs = mkrs(ws, WS_END);
  asm volatile("" : "+v"(xa_), "+v"(xo_), "+v"(hw), "+v"(bact), "+v"(a128), "+v"(syn), "+v"(o_qs), "+v"(one), "+v"(T4.m), "+v"(sa));
  asm volatile("s_waitcnt lgkmcnt(0)" ::"s"(pf0), "s"(pf1), "s"(pf2));
  await<C_X, NLW>(syn);
  ST(1);
  {
    const v4u xa = *(LAS v4u *)xa_, xb = *(LAS v4u *)(xa_ + 1024);
    const v2u xo = *(LAS v2u *)xo_;
    f32x2 v[8];
#pragma unroll
    for (int i = 0; i < 4; ++i) {
      v[i] = f32x2{blo(xa[i]), bhi(xa[i])};
      v[4 + i] = f32x2{blo(xb[i]), bhi(xb[i])};
    }
    const f32x2 s = ((v[0] + v[1]) + (v[2] + v[3])) + ((v[4] + v[5]) + (v[6] + v[7]));
    const float mean = sum64(s.x + s.y) * (1.f / 1024.f);
    f32x2 qq = {0.f, 0.f};
#pragma unroll
    for (int i = 0; i < 8; ++i) qq += (v[i] - mean) * (v[i] - mean);
    const float var = rbf(sum64(qq.x + qq.y) * (1.f / 1024.f));
    const float m = rbf(mean), ri = __builtin_amdgcn_rcpf(rbf(__builtin_amdgcn_sqrtf(rbf(var + 1e-5f))));
    const unsigned d0 = pkbf(blo(xo.x) - m, bhi(xo.x) - m), d1 = pkbf(blo(xo.y) - m, bhi(xo.y) - m);
    *(LAS v2u *)hw = v2u{pkbf(blo(d0) * ri, bhi(d0) * ri), pkbf(blo(d1) * ri, bhi(d1) * ri)};
  }
  arrive<C_XN>(sa, one);
  ST(2);
  unsigned Ev;
  if (w < 4) {
    await<C_XN, NCW>(syn);
    v8s af[8];
    v4u bf[8];
    rd_b<8>(bact, bf);
    await<C_Q, NLW>(syn);
    Ev = *(LAS unsigned *)(syn + (L_EP - L_SYNC));
    rd_a<8, 4096>(a128, af);
    SCHED();
    const unsigned E0 = __builtin_amdgcn_readfirstlane(Ev);
    T4.t0 = E0 + 1u, T4.t1 = (E0 >> 8) + 1u, T4.t2 = (E0 >> 16) + 1u, T4.t3 = (E0 >> 24) + 1u;
    stws(rs, o_qs, tag4(mm<8>(af, bf), T4));
  } else {
    await<C_Q, NLW, 2>(syn);
    Ev = *(LAS unsigned *)(syn + (L_EP - L_SYNC));
  }
  ST(3);
  const unsigned E = __builtin_amdgcn_readfirstlane(Ev);
  const unsigned Tw = ((E & 0x7f7f7f7fu) + 0x01010101u) ^ (E & 0x80808080u);
  T.t0 = Tw & 0xffu, T.t1 = (Tw >> 8) & 0xffu, T.t2 = (Tw >> 16) & 0xffu, T.t3 = Tw >> 24;
  T4.t0 = T.t0 & 0xfu, T4.t1 = T.t1 & 0xfu, T4.t2 = T.t2 & 0xfu, T4.t3 = T.t3 & 0xfu;
  SCHED();
  const int rr = xcd * 4 + kq, rb = 2 * (rr >> 3) + ((l >> 2) & 1), rcol = (rr & 7) * 128 + 32 * (w & 3) + 16 * ((l >> 3) & 1) + 4 * p4;
  const int dcu = xcd * 28 + j, eu = (w == 0) ? dcu : 224 + dcu;
  const bool hasE = (w == 0) || (w == 1 && dcu < 32);
  const int drow = 16 * ((trow >> 1) & 3) + 2 * (g >> 1) + (trow & 1);
  LAS unsigned char *a256 = L + L_R2 + (32 * g + q4) * 256 + (p4 & 1) * 8 + (((2 * w + (p4 >> 1)) ^ sw256) * 16);
  LAS unsigned char *kp[4];
#pragma unroll
  for (int i = 0; i < 4; ++i) kp[i] = L + L_R1 + (32 * q4 + 4 * w + p4) * 256 + (((4 * i + g) ^ col) * 16);
  LAS unsigned char *qw = L + L_ACT2 + ((l >> 3) & 3) * 512 + ((l >> 1) & 3) * 128 + w * 16 + (l & 1) * 8 + (l >> 5) * 4;
  LAS unsigned char *mxw = L + (l < 8 ? L_MX + l * 32 + w * 4 : L_JUNK + 256 + 4 * l), *mxr = L + L_MX + b8 * 32;
  LAS unsigned char *pw = col < 8 ? bact + (w >> 1) * 512 + (w & 1) * 8 : L + L_JUNK + 1024 + 8 * l;
  LAS unsigned char *ow = L + L_ACT2 + ((l >> 3) & 3) * 512 + p4 * 128 + w * 16 + ((l >> 2) & 1) * 8 + (l >> 5) * 4;
  LAS unsigned char *dw = L + L_ACT2 + (w & 1) * 512 + p4 * 128 + (2 * (w >> 1) + ((l >> 2) & 1)) * 16 + (col >> 3) * 8 + g * 2;
  LAS unsigned char *ad0 = L + L_R2 + drow * 512 + (p4 & 1) * 8 + (((4 * w + (p4 >> 1)) ^ sw256) * 16);
  LAS unsigned char *ad1 = L + L_R2 + drow * 512 + (p4 & 1) * 8 + (((4 * w + 2 + (p4 >> 1)) ^ sw256) * 16);
  int o_qg = WS_QP + ((akv * 4 + (ab >> 1)) * 8 + w) * 4096 + (l >> 2) * 128 + (ab & 1) * 64 + p4 * 16;
  int o_as = WS_AP + (((akv * 8 + (b8 >> 1) * 2 + (w >> 2)) * 64 + ab * 8 + as) * 4 + (w & 3)) * 128 + lst + dead;
  int o_ms = WS_ML + (akv * 64 + ab * 8 + as) * 128 + l * 16 + (l < 8 ? 0 : OOB);
  int o_mg = WS_ML + ((ku >> 3) * 64 + w * 8 + 4 * (l >> 5) + p4) * 128 + (2 * ((ku >> 1) & 3) + ((l >> 2) & 1)) * 16;
  int o_ag = WS_AP + (ku * 64 + w * 8 + 4 * (l >> 5)) * 512 + (l & 31) * 16;
  int o_os = WS_OP + ((((b8 >> 1) * 32 + ocg * 4 + (w >> 1)) * 32 + ku) * 2 + (w & 1)) * 128 + lst + dead;
  int o_s2 = isG ? WS_GP + ((fg * 4 + kq) * 8 + w) * 512 + b8 * 64 + g * 16 + dead : WS_HB + (rb * 1024 + rcol) * 4 + (l < 16 && w < 4 ? 0 : OOB);
  int o_g2 = isG ? WS_HB + w * 4096 + l * 16 : WS_OP + (rr * 4 + w) * 8192 + l * 16;
  int o_ds = isG ? WS_DP + ((b8 * 32 + 8 * kq + w) * 56 + fg) * 128 + (col >> 3) * 64 + g * 16 : OOB;
  int o_gg = WS_GP + ((fg * 4 + g) * 8 + 2 * (w & 1) + (col >> 3)) * 512 + (w >> 1) * 128 + b8 * 16;
  int o_eh = WS_HB + eu * 128 + b8 * 16, o_dg = WS_DP + eu * 7168 + l * 16, o_out = (eu * 32 + 4 * b8 + g) * 2 + dead;
  v2u xr = *(LAS v2u *)(L + L_X + (rb * 1024 + rcol) * 2);
  asm volatile("" : "+v"(xr), "+v"(a256), "+v"(kp[0]), "+v"(kp[1]), "+v"(kp[2]), "+v"(kp[3]), "+v"(qw), "+v"(mxw), "+v"(mxr), "+v"(pw), "+v"(ow),
               "+v"(dw), "+v"(ad0), "+v"(ad1));
  asm volatile("" : "+v"(o_qg), "+v"(o_as), "+v"(o_ms), "+v"(o_mg), "+v"(o_ag), "+v"(o_os), "+v"(o_s2), "+v"(o_g2), "+v"(o_ds), "+v"(o_gg),
               "+v"(o_eh), "+v"(o_dg), "+v"(o_out), "+v"(T.m));
  const v4u bad = {~T.t0, ~T.t1, ~T.t2, ~T.t3};
  stws(rs, o_as, bad);
  await<C_K, NLW, 1>(syn);
  __builtin_amdgcn_s_sleep(DLY_Q);
  float M;
  v4u pf[4];
  {
    v8s kf[4];
#pragma unroll
    for (int i = 0; i < 4; ++i) kf[i] = *(LAS v8s *)kp[i];
    f32x4 a[2];
    gather<2, 2048, 0xfu>(rs, o_qg, a, T4);
    ST(4);
    const f32x4 s = a[0] + a[1];
    *(LAS unsigned *)qw = pkbf(rs32(s.x, s.z), rs32(s.y, s.w));
    arrive<C_QL>(sa, one);
    await<C_QL, NCW>(syn);
    ST(5);
    v4u qf[4];
    rd_b<4>(bact + 4096, qf);
    const f32x4 acc = mm<4>(kf, qf);
    const unsigned r0 = pkbf(acc.x, acc.y), r1 = pkbf(acc.z, acc.w);
    const float SCL = 0.08838834764831845f;
    const unsigned s0 = pkbf(blo(r0) * SCL, bhi(r0) * SCL), s1 = pkbf(blo(r1) * SCL, bhi(r1) * SCL);
    const float sc0 = blo(s0), sc1 = bhi(s0), sc2 = blo(s1), sc3 = bhi(s1);
    *(LAS float *)mxw = mx32(mx16(fmx(fmx(sc0, sc1), fmx(sc2, sc3))));
    arrive<C_MX>(sa, one);
    await<C_MX, NCW>(syn);
    const f32x4 m0 = *(LAS f32x4 *)mxr, m1 = *(LAS f32x4 *)(mxr + 16);
    M = fmx(fmx(fmx(m0.x, m0.y), fmx(m0.z, m0.w)), fmx(fmx(m1.x, m1.y), fmx(m1.z, m1.w)));
    const float p0 = __builtin_amdgcn_exp2f((sc0 - M) * LOG2E), p1 = __builtin_amdgcn_exp2f((sc1 - M) * LOG2E),
                p2 = __builtin_amdgcn_exp2f((sc2 - M) * LOG2E), p3 = __builtin_amdgcn_exp2f((sc3 - M) * LOG2E);
    *(LAS v2u *)pw = v2u{pkbf(p0, p1), pkbf(p2, p3)};
    float ls = (p0 + p1) + (p2 + p3);
    ls = rs16(ls, ls);
    *(LAS float *)(mxw + 256) = rs32(ls, ls);
    arrive<C_P>(sa, one);
    await<C_P, NCW>(syn);
    rd_b<4>(bact, pf);
    if (w == 7) {
      const f32x4 l2 = *(LAS f32x4 *)(mxr + 256) + *(LAS f32x4 *)(mxr + 272);
      stws(rs, o_ms, tag4(f32x4{M, (l2.x + l2.y) + (l2.z + l2.w), 0.f, 0.f}, T));
    }
  }
  ST(6);
  await<C_V, NLW>(syn);
  {
    v8s af[4];
    rd_a<4, 2048>(a256, af);
    stws(rs, o_as, tag4(mm<4>(af, pf), T));
  }
  ST(7);
  stws(rs, o_os, bad);
  stws(rs, o_s2, bad);
  stws(rs, o_ds, bad);
  {
    f32x4 ml[1];
    gather<1, 0, 0xffu, true>(rs, o_mg, ml, T);
    float mm_ = fmx(ml[0].x, dpp<XOR1>(ml[0].x));
    mm_ = mx32(fmx(mm_, dpp<XOR2>(mm_)));
    const float wt = __builtin_amdgcn_exp2f((ml[0].x - mm_) * LOG2E);
    float ll = wt * ml[0].y;
    ll += dpp<XOR1>(ll);
    ll += dpp<XOR2>(ll);
    ll = rs32(ll, ll);
    const float cf = wt * __builtin_amdgcn_rcpf(ll);
    float c0 = dpp<0x00>(cf), c1 = dpp<0x55>(cf), c2 = dpp<0xAA>(cf), c3 = dpp<0xFF>(cf);
    asm volatile("" : "+v"(c0), "+v"(c1), "+v"(c2), "+v"(c3));
    await<C_WO, NLW>(syn);
    ST(8);
    v8s af[4];
    rd_a<4, 2048>(a256 + 32768, af);
    f32x4 a[4];
    gather<4, 512>(rs, o_ag, a, T);
    ST(9);
    const f32x4 o = (a[0] * c0 + a[1] * c1) + (a[2] * c2 + a[3] * c3);
    *(LAS unsigned *)ow = pkbf(rs32(o.x, o.z), rs32(o.y, o.w));
    BAR();
    v4u bf[4];
    rd_b<4>(bact + 4096, bf);
    stws(rs, o_os, tag4(mm<4>(af, bf), T));
  }
  ST(10);
  if (!isG) {
    if (w < 4) {
      __builtin_amdgcn_s_sleep(DLY_R);
      f32x4 a[8];
      gather<8, 1024>(rs, o_g2, a, T);
      ST(11);
      f32x4 s = ((a[0] + a[1]) + (a[2] + a[3])) + ((a[4] + a[5]) + (a[6] + a[7]));
#pragma unroll
      for (int i = 0; i < 4; ++i) {
        const float t = rs16(s[i], s[i]);
        s[i] = rs32(t, t);
      }
      const unsigned t0 = pkbf(s.x, s.y), t1 = pkbf(s.z, s.w);
      const unsigned h0 = pkbf(blo(xr.x) + blo(t0), bhi(xr.x) + bhi(t0)), h1 = pkbf(blo(xr.y) + blo(t1), bhi(xr.y) + bhi(t1));
      stws(rs, o_s2, v4u{(h0 << 16) | T.t0, (h0 & 0xffff0000u) | T.t1, (h1 << 16) | T.t2, (h1 & 0xffff0000u) | T.t3});
      ST(12);
    }
  } else {
    await<C_H, NLW>(syn);
    {
      v8s af[8];
      rd_a<8, 4096>(a128, af);
      f32x4 h[4];
      gather<4, 1024>(rs, o_g2, h, T);
      ST(11);
      const f32x4 sv = (h[0] + h[1]) + (h[2] + h[3]);
      const float mean = sum64((sv.x + sv.y) + (sv.z + sv.w)) * (1.f / 1024.f);
      const f32x4 d0 = h[0] - mean, d1 = h[1] - mean, d2 = h[2] - mean, d3 = h[3] - mean;
      const f32x4 qv = (d0 * d0 + d1 * d1) + (d2 * d2 + d3 * d3);
      const float var = rbf(sum64((qv.x + qv.y) + (qv.z + qv.w)) * (1.f / 1024.f));
      const float m = rbf(mean), ri = __builtin_amdgcn_rcpf(rbf(__builtin_amdgcn_sqrtf(rbf(var + 1e-5f))));
      const f32x4 ho = (kq & 2) ? ((kq & 1) ? h[3] : h[2]) : ((kq & 1) ? h[1] : h[0]);
      const unsigned e0 = pkbf(ho.x - m, ho.y - m), e1 = pkbf(ho.z - m, ho.w - m);
      *(LAS v2u *)hw = v2u{pkbf(blo(e0) * ri, bhi(e0) * ri), pkbf(blo(e1) * ri, bhi(e1) * ri)};
      BAR();
      ST(12);
      v4u bf[8];
      rd_b<8>(bact, bf);
      stws(rs, o_s2, tag4(mm<8>(af, bf), T));
    }
    if (c == 0 && w == 2 && l == 0) __hip_atomic_store((GAS unsigned *)ws, Tw, __ATOMIC_RELAXED, __HIP_MEMORY_SCOPE_AGENT);
    float hv = 0.f;
    if (hasE) {
      f32x4 hE[1];
      gather<1, 0>(rs, o_eh, hE, T);
      hv = (g & 2) ? ((g & 1) ? hE[0].w : hE[0].z) : ((g & 1) ? hE[0].y : hE[0].x);
    }
    __builtin_amdgcn_s_sleep(DLY_D);
    {
      v8s a0[2], a1[2];
      rd_a<2, 4096>(ad0, a0);
      rd_a<2, 4096>(ad1, a1);
      f32x4 a[2];
      gather<2, 2048>(rs, o_gg, a, T);
      ST(13);
      const unsigned gu = pkbf(rs16(rs32(a[0].x, a[0].z), rs32(a[0].y, a[0].w)), rs16(rs32(a[1].x, a[1].z), rs32(a[1].y, a[1].w)));
      const float sg = rbf(__builtin_amdgcn_rcpf(1.f + __builtin_amdgcn_exp2f(blo(gu) * -LOG2E)));
      *(LAS u16 *)dw = (u16)pkbf(sg * bhi(gu), 0.f);
      BAR();
      v4u bf[2];
      rd_b<2>(bact + 4096, bf);
      SCHED();
      f32x4 c0 = {0.f, 0.f, 0.f, 0.f}, c1 = {0.f, 0.f, 0.f, 0.f};
      c0 = mfma(a0[0], bf[0], c0);
      c1 = mfma(a1[0], bf[0], c1);
      c0 = mfma(a0[1], bf[1], c0);
      c1 = mfma(a1[1], bf[1], c1);
      stws(rs, o_ds, tag4(col < 8 ? c0 : c1, T));
    }
    ST(14);
    if (hasE) {
      __builtin_amdgcn_s_sleep(DLY_E);
      f32x4 a[7];
      gather<7, 1024>(rs, o_dg, a, T);
      const f32x4 s = ((a[0] + a[1]) + (a[2] + a[3])) + ((a[4] + a[5]) + a[6]);
      float t = rs16(rs32(s.x, s.z), rs32(s.y, s.w));
      t += dpp<XOR8>(t);
      __builtin_amdgcn_raw_buffer_store_b16((u16)pkbf(hv + rbf(t), 0.f), mkrs(out, 8 * 1024 * 2), o_out, 0, 0);
    }
  }
  STOUT();
}

extern "C" void kernel_launch(void *const *d_in, const int *in_sizes, int n_in, void *d_out, int out_size, void *d_ws, size_t ws_size,
    hipStream_t stream) {
  (void)in_sizes, (void)n_in, (void)out_size, (void)ws_size;
  (void)hipFuncSetAttribute((const void *)blk_kernel, hipFuncAttributeMaxDynamicSharedMemorySize, L_END);
  blk_kernel<<<256, NTHR, L_END, stream>>>((const u16 *)d_in[0], (const u16 *)d_in[3], (const u16 *)d_in[1], (const u16 *)d_in[2],
      (const u16 *)d_in[4], (const u16 *)d_in[6], (const u16 *)d_in[7], (const u16 *)d_in[8], (u16 *)d_out, (unsigned char *)d_ws);
}
```

```cpp
#include <hip/hip_runtime.h>

#define GAS __attribute__((address_space(1)))
#define LAS __attribute__((address_space(3)))
typedef unsigned short u16;
typedef unsigned long long u64;
typedef unsigned v4u __attribute__((ext_vector_type(4)));
typedef unsigned v2u __attribute__((ext_vector_type(2)));
typedef float f32x4 __attribute__((ext_vector_type(4)));
typedef short v4s __attribute__((ext_vector_type(4)));
typedef short v8s __attribute__((ext_vector_type(8)));
typedef __bf16 v8bf __attribute__((ext_vector_type(8)));
typedef __bf16 v2bf __attribute__((ext_vector_type(2)));
typedef float f32x2 __attribute__((ext_vector_type(2)));

#define AUXW 2
#define WQX 3
#define W_XK 4
#define W_XO 4
#define NO1 8
#define W_XO2 8
#define W_YK 8
#define W_YG 5
#define NG1 6
#define W_YU 8
#define TQX 3
#define TQY 6
#define W_XD 10
#define ND0 4
#define DLY_W 4
#define DLY_RY 30
#define DLY_N 40
#define SLP_M 4
#define DLY_M 0
#define SLP_P 2
#define DLY_R 22
#define D_H 175
#define D_W 30
#define DLY_E 24

namespace {
constexpr int NCW = 8, NLW = 4, NTHR = (NCW + NLW) * 64;
constexpr int WS_QP = 4096;
constexpr int WS_AP = WS_QP + 4 * 8 * 4096 * 4;
constexpr int WS_ML = WS_AP + 32 * 256 * 128;
constexpr int WS_OP = WS_ML + 256 * 128;
constexpr int WS_HB = WS_OP + 32 * 8 * 1024 * 4;
constexpr int WS_GP = WS_HB + 8 * 1024 * 4;
constexpr int WS_CS = WS_GP + 56 * 16384;
constexpr int WS_DP = WS_CS + 56 * 4096;
constexpr int WS_END = WS_DP + 56 * 8 * 1024 * 4;
constexpr int WS_STAMP = (WS_END + 4095) / 4096 * 4096;
constexpr int OOB = 0x40000000;
constexpr int L_K = 32768, L_H2 = 65536, L_X = 131072, L_ACT = L_X + 16384, L_ACT2 = L_ACT + 4096, L_MX = L_ACT2 + 2048, L_LS = L_MX + 256,
              L_SYNC = L_LS + 256, L_EP = L_SYNC + 64, L_JUNK = L_EP + 256, L_END = L_JUNK + 2048;
enum { C_X = 0, C_XN, C_Q1, C_Q, C_K, C_TQ, C_QL, C_MX, C_P, C_V, C_G, C_R0, C_R1, C_TA, C_N };
constexpr int SC1 = 16;
constexpr unsigned SPIN_CAP = 1u << 16;
constexpr float LOG2E = 1.4426950408889634f;

#define BAR() asm volatile("s_waitcnt lgkmcnt(0)\n\ts_barrier" ::: "memory")
#define VMWAIT(n) asm volatile("s_waitcnt vmcnt(%0)" ::"n"(n) : "memory")
#define SCHED() __builtin_amdgcn_sched_barrier(0)
#define PIN(x) asm volatile("" : "+v"(x))

__device__ __forceinline__ float asf(unsigned u) { return __builtin_bit_cast(float, u); }
__device__ __forceinline__ unsigned asu(float f) { return __builtin_bit_cast(unsigned, f); }
__device__ __forceinline__ unsigned pkbf(float lo, float hi) {
  f32x2 v = {lo, hi};
  return __builtin_bit_cast(unsigned, __builtin_convertvector(v, v2bf));
}
__device__ __forceinline__ float blo(unsigned p) { return asf(p << 16); }
__device__ __forceinline__ float bhi(unsigned p) { return asf(p & 0xffff0000u); }
__device__ __forceinline__ float rbf(float f) { return blo(pkbf(f, f)); }
__device__ __forceinline__ float fmx(float a, float b) { return __builtin_fmaxf(a, b); }
template <int CTRL> __device__ __forceinline__ float dpp(float x) {
  return asf(__builtin_amdgcn_mov_dpp(asu(x), CTRL, 0xf, 0xf, true));
}
constexpr int XOR1 = 0xB1, XOR2 = 0x4E, XOR7 = 0x141, XOR8 = 0x128;
__device__ __forceinline__ float rs32(float a, float b) {
  auto r = __builtin_amdgcn_permlane32_swap(asu(a), asu(b), false, false);
  return asf(r[0]) + asf(r[1]);
}
__device__ __forceinline__ float rs16(float a, float b) {
  auto r = __builtin_amdgcn_permlane16_swap(asu(a), asu(b), false, false);
  return asf(r[0]) + asf(r[1]);
}
__device__ __forceinline__ float mx32(float a) {
  auto r = __builtin_amdgcn_permlane32_swap(asu(a), asu(a), false, false);
  return fmx(asf(r[0]), asf(r[1]));
}
__device__ __forceinline__ float mx16(float a) {
  auto r = __builtin_amdgcn_permlane16_swap(asu(a), asu(a), false, false);
  return fmx(asf(r[0]), asf(r[1]));
}
__device__ __forceinline__ float sum64(float x) {
  x += dpp<XOR1>(x);
  x += dpp<XOR2>(x);
  x += dpp<XOR7>(x);
  x += dpp<XOR8>(x);
  x = rs16(x, x);
  return rs32(x, x);
}
__device__ __forceinline__ float fmx3(float a, float b, float c) {
  float r;
  asm("v_max3_f32 %0, %1, %2, %3" : "=v"(r) : "v"(a), "v"(b), "v"(c));
  return r;
}
__device__ __forceinline__ f32x4 score(f32x4 acc) {
  const float SCL = 0.08838834764831845f;
  const unsigned r0 = pkbf(acc.x, acc.y), r1 = pkbf(acc.z, acc.w);
  const unsigned s0 = pkbf(blo(r0) * SCL, bhi(r0) * SCL), s1 = pkbf(blo(r1) * SCL, bhi(r1) * SCL);
  return f32x4{blo(s0), bhi(s0), blo(s1), bhi(s1)};
}
__device__ __forceinline__ float prob(f32x4 s, float M, LAS unsigned char *pw) {
  const float p0 = __builtin_amdgcn_exp2f((s.x - M) * LOG2E), p1 = __builtin_amdgcn_exp2f((s.y - M) * LOG2E),
              p2 = __builtin_amdgcn_exp2f((s.z - M) * LOG2E), p3 = __builtin_amdgcn_exp2f((s.w - M) * LOG2E);
  *(LAS v2u *)pw = v2u{pkbf(p0, p1), pkbf(p2, p3)};
  return (p0 + p1) + (p2 + p3);
}
struct Tag {
  unsigned t0, t1, t2, t3;
  unsigned m;
  mutable unsigned n;
};
__device__ __forceinline__ v4u tag4(f32x4 v, const Tag &T) {
  v4u o;
  o.x = (T.m & T.t0) | (~T.m & asu(v.x));
  o.y = (T.m & T.t1) | (~T.m & asu(v.y));
  o.z = (T.m & T.t2) | (~T.m & asu(v.z));
  o.w = (T.m & T.t3) | (~T.m & asu(v.w));
  return o;
}
#define LDX
#define STX
#define AUXS SC1
__device__ __forceinline__ v4u ldws(__amdgpu_buffer_rsrc_t rs, int off) { return __builtin_amdgcn_raw_buffer_load_b128(rs, off LDX, 0, SC1); }
__device__ __forceinline__ void stws(__amdgpu_buffer_rsrc_t rs, int off, v4u v) { __builtin_amdgcn_raw_buffer_store_b128(v, rs, off STX, 0, AUXS); }
__device__ __forceinline__ f32x4 mfma(v8s a, v4u b, f32x4 c) {
  return __builtin_amdgcn_mfma_f32_16x16x32_bf16(__builtin_bit_cast(v8bf, a), __builtin_bit_cast(v8bf, b), c, 0, 0, 0);
}
__device__ __forceinline__ v8s trfrag(LAS unsigned char *p, int off0, int off1) {
  const v4s a = __builtin_amdgcn_ds_read_tr16_b64_v4i16((LAS v4s *)(p + off0));
  const v4s b = __builtin_amdgcn_ds_read_tr16_b64_v4i16((LAS v4s *)(p + off1));
  return __builtin_shufflevector(a, b, 0, 1, 2, 3, 4, 5, 6, 7);
}
template <int NK, int KS> __device__ __forceinline__ void rd_a(LAS unsigned char *a, v8s (&af)[NK]) {
#pragma unroll
  for (int ks = 0; ks < NK; ++ks) af[ks] = trfrag(a, ks * KS, ks * KS + KS / 2);
}
template <int NK> __device__ __forceinline__ void rd_b(LAS unsigned char *b, v4u (&bf)[NK]) {
#pragma unroll
  for (int ks = 0; ks < NK; ++ks) bf[ks] = *(LAS v4u *)(b + ks * 512);
}
template <int NK> __device__ __forceinline__ f32x4 mm(const v8s (&af)[NK], const v4u (&bf)[NK]) {
  SCHED();
  f32x4 acc = {0.f, 0.f, 0.f, 0.f};
#pragma unroll
  for (int ks = 0; ks < NK; ++ks) acc = mfma(af[ks], bf[ks], acc);
  return acc;
}
template <int NK> __device__ __forceinline__ f32x4 mm2(const v8s (&a0)[NK], const v8s (&a1)[NK], const v4u (&bf)[NK], bool first) {
  f32x4 c0 = {0.f, 0.f, 0.f, 0.f}, c1 = {0.f, 0.f, 0.f, 0.f};
#pragma unroll
  for (int ks = 0; ks < NK; ++ks) {
    c0 = mfma(a0[ks], bf[ks], c0);
    c1 = mfma(a1[ks], bf[ks], c1);
  }
  return first ? c0 : c1;
}
template <int NK> __device__ __forceinline__ void pin_a(v8s (&af)[NK]) {
#pragma unroll
  for (int ks = 0; ks < NK; ++ks) PIN(af[ks]);
}
template <int N, int STEP> __device__ __forceinline__ void issue(__amdgpu_buffer_rsrc_t rs, int off, v4u (&a)[N]) {
  asm volatile("" ::: "memory");
#pragma unroll
  for (int i = 0; i < N; ++i) a[i] = ldws(rs, off + i * STEP);
}
template <int N, unsigned MASK> __device__ __forceinline__ bool stale(const v4u (&a)[N], f32x4 (&v)[N], const Tag &T) {
  unsigned d = 0;
#pragma unroll
  for (int i = 0; i < N; ++i) {
    const v4u u = {a[i].x ^ T.t0, a[i].y ^ T.t1, a[i].z ^ T.t2, a[i].w ^ T.t3};
    d |= (u.x | u.y) | (u.z | u.w);
    v[i] = __builtin_bit_cast(f32x4, u);
  }
  return __builtin_amdgcn_ballot_w64((d & MASK) != 0u) != 0ull;
}
template <int N, int STEP, unsigned MASK = 0xffu>
__device__ __forceinline__ void settle(__amdgpu_buffer_rsrc_t rs, int off, const v4u (&a)[N], f32x4 (&v)[N], const Tag &T) {
  T.n = 0;
  if (__builtin_expect(stale<N, MASK>(a, v, T), 0)) {
    unsigned spins = 0;
    bool again;
#pragma unroll 1
    do {
      v4u r[N];
      __builtin_amdgcn_s_sleep(SLP_P);
      issue<N, STEP>(rs, off, r);
      again = stale<N, MASK>(r, v, T);
    } while (again && ++spins < SPIN_CAP);
    T.n = spins + 1;
  }
}
template <int N, int STEP, unsigned MASK = 0xffu>
__device__ __forceinline__ void gather(__amdgpu_buffer_rsrc_t rs, int off, f32x4 (&v)[N], const Tag &T) {
  v4u a[N];
  issue<N, STEP>(rs, off, a);
  settle<N, STEP, MASK>(rs, off, a, v, T);
}
__device__ __forceinline__ unsigned now() { return (unsigned)__builtin_amdgcn_s_memtime(); }
constexpr int TK = 22;
__device__ __forceinline__ void until(unsigned t) {
  while ((int)(now() - t) < 0) {
  }
}
__device__ __forceinline__ __amdgpu_buffer_rsrc_t mkrs(const void *p, int bytes) {
  return __builtin_amdgcn_make_buffer_rsrc((void *)p, 0, bytes, 0x00020000);
}
template <int AUX, int IMM = 0> __device__ __forceinline__ void dma1(__amdgpu_buffer_rsrc_t rs, LAS unsigned char *dst, int voff, int soff) {
  __builtin_amdgcn_raw_ptr_buffer_load_lds(rs, (LAS void *)dst, 16, voff, soff, IMM, AUX);
}
template <int K> __device__ __forceinline__ void arrive(int sa, unsigned one = 1u) {
  asm volatile("ds_add_u32 %0, %1 offset:%2" ::"v"(sa), "v"(one), "n"(4 * K) : "memory");
}
template <int K, int N, int SLEEP = 0, typename P> __device__ __forceinline__ void await(P syn) {
  unsigned v, left, t;
  asm volatile("s_mov_b32 %1, 0x1000000\n"
               ".Lspin%=:\n\t"
               "ds_read_b32 %0, %3 offset:%4\n\t"
               ".if %6\n\ts_sleep %6\n\t.endif\n\t"
               "s_sub_u32 %1, %1, 1\n\t"
               "s_cbranch_scc1 .Ldone%=\n\t"
               "s_waitcnt lgkmcnt(0)\n\t"
               "v_readfirstlane_b32 %2, %0\n\t"
               "s_cmp_lt_u32 %2, %5\n\t"
               "s_cbranch_scc1 .Lspin%=\n"
               ".Ldone%=:\n\t"
               "s_waitcnt lgkmcnt(0)"
               : "=&v"(v), "=&s"(left), "=&s"(t)
               : "v"(syn), "n"(4 * K), "n"(N), "n"(SLEEP)
               : "memory", "scc");
}
#define await_l await
template <int NP, int W, int LANDED, int MODE = 0, int LI = W - 1, int LO = 0>
__device__ __forceinline__ void tile(__amdgpu_buffer_rsrc_t rs, LAS unsigned char *dst, int voff, int soff, int sstep, int sa) {
#pragma unroll 1
  for (int i = LO; i < NP; ++i) {
    if (W < 64) VMWAIT(W - 1);
    if (LANDED >= 0 && i == LI) {
      VMWAIT(LI < 63 ? LI : 63);
      arrive<(LANDED < 0 ? 0 : LANDED)>(sa);
    }
    if (MODE == 0) dma1<AUXW>(rs, dst + i * 1024, voff, soff + i * sstep);
    if (MODE == 2) dma1<AUXW>(rs, dst + i * 1024, voff ^ ((i & 1) * 32), soff + ((i >> 1) * 8 + (i & 1)) * 2048);
    if (MODE == 1) dma1<AUXW>(rs, dst + i * 1024, voff, soff + (((i >> 1) & 1) * 128 + (i >> 2) * 1024 + (i & 1) * 8) * 2048);
  }
}
#define RETRY(i)
#define ST(i)
#define STOUT()
}

#define FRESH(v) asm volatile("" : "+s"(v))

__global__ void __launch_bounds__(NTHR, 3)
    blk_kernel(const u16 *x, const u16 *wq, const u16 *kc, const u16 *vc, const u16 *wo, const u16 *wg, const u16 *wu, const u16 *wd, u16 *out,
        unsigned char *ws) {
  extern __shared__ __attribute__((aligned(16))) unsigned char smem_[];
  LAS unsigned char *L = (LAS unsigned char *)smem_;
  const int tid = threadIdx.x, l = tid & 63, wv = __builtin_amdgcn_readfirstlane(tid >> 6);
  int c = blockIdx.x;
  const bool isX = (c & 8) == 0;
#define XCD (c & 7)
#define JJ (c >> 4)
#define UNITS()                                                                                                         \
  const int xcd = XCD, j = c >> 3, jj = JJ;                                                                             \
  const int kq = j & 3, cg64 = (j >> 2) * 8 + ((xcd + (j >> 2)) & 7);            \
  const int akv = j >> 3, as = j & 7;                                     \
  const int ku = 4 * xcd + (jj >> 2), ocq = jj & 3;                              \
  const bool mlp = jj < 14;                                                                                             \
  const int fg = (jj >> 1) * 8 + xcd, hf = jj & 1;                       \
  const int yu = jj * 8 + xcd, gq = yu / 28, pg = yu - 28 * gq, pu = pg < 26 ? pg + 2 : pg - 26;    \
  (void)kq, (void)cg64, (void)akv, (void)as, (void)ku, (void)ocq, (void)mlp, (void)fg, (void)hf, (void)gq, (void)pu;

  const int lw = wv & 3;
  asm volatile("ds_write_b32 %0, %1" ::"v"(L_SYNC + 4 * (l & 15)), "v"(0u) : "memory");
  int vx = l * 16, vq = (l >> 3) * 8192 + ((l & 7) ^ (((l >> 4) & 3) << 1)) * 16;
  int sq = (((c >> 3) & 3) * 256 + lw * 64) * 8192 + ((c >> 5) * 8 + ((XCD + (c >> 5)) & 7)) * 128, sx = lw * 4096;
  LAS unsigned char *dq = L + lw * 8192, *dx = L + L_X + lw * 4096;
  asm volatile("" : "+v"(vx), "+v"(vq), "+s"(sq), "+s"(sx), "+s"(dq), "+s"(dx));
  asm volatile("s_waitcnt lgkmcnt(0)\n\ts_barrier" ::"s"(x), "s"(wq), "s"(kc), "s"(vc), "s"(wo), "s"(wg), "s"(wu), "s"(wd), "s"(out), "s"(ws)
               : "memory");
  if (__builtin_expect(wv < NLW, 1)) {
    __builtin_amdgcn_s_waitcnt(0x0F70);
    {
      const __amdgpu_buffer_rsrc_t rx = mkrs(x, 8 * 1024 * 2);
      dma1<0, 0>(rx, dx, vx, sx);
      dma1<0, 1024>(rx, dx, vx, sx);
      dma1<0, 2048>(rx, dx, vx, sx);
      dma1<0, 3072>(rx, dx, vx, sx);
    }
    tile<8, 64, -1>(mkrs(wq, 1024 * 4096 * 2), dq, vq, sq, 8 * 8192, 0);
    const int sa = l == 0 ? L_SYNC : L_JUNK + 4 * l;
    VMWAIT(8);
    arrive<C_X>(sa);
    __builtin_amdgcn_raw_ptr_buffer_load_lds(mkrs(ws, 4096), (LAS void *)(L + L_EP), 4, 0, 0, 0, SC1);
    ST(2);
    FRESH(c);
    UNITS();
    const __amdgpu_buffer_rsrc_t rk = mkrs(kc, 8 * 4 * 4096 * 128 * 2), rv = mkrs(vc, 8 * 4 * 4096 * 128 * 2);
    const int r4 = l >> 4;
    const int v256 = ((l & 15) ^ ((4 * (lw & 1) + r4) << 1)) * 16, vv = r4 * 256 + v256, vk = r4 * 256 + (((l & 15) ^ (4 * lw + r4)) * 16);
    const int kv0 = ((xcd * 4 + akv) * 4096 + 3072 + (as >> 1) * 256) * 256;
    LAS unsigned char *d0 = L + lw * 16384;
    if (!isX) {
      const int kvb = kv0 + (64 + lw * 48) * 256;
      tile<WQX + 1, WQX + 1, C_Q>(rk, L + L_K + lw * 12288, vk, kvb, 1024, sa);
      tile<12, W_YK, -1, 0, 0, WQX + 1>(rk, L + L_K + lw * 12288, vk, kvb, 1024, sa);
      ST(3);
      tile<TQY, W_YK, C_K>(rv, L + L_K + 49152 + lw * 12288, vv, kvb, 1024, sa);
      ST(4);
      arrive<C_TQ>(sa);
      BAR();
      tile<12, W_YK, C_K, 0, W_YK - 1, TQY>(rv, L + L_K + 49152 + lw * 12288, vv, kvb, 1024, sa);
      ST(5);
      if (mlp) {
        const int sg = (gq * 256 + lw * 4) * 7168, vg = r4 * 7168 + v256;
        await_l<C_R0, NCW>(L_SYNC);
        tile<NG1, W_YG, C_V>(mkrs(wg, 1024 * 3584 * 2), d0, vg, sg + pg * 256, 16 * 7168, sa);
        tile<16, W_YU, -1, 0, 0, NG1>(mkrs(wg, 1024 * 3584 * 2), d0, vg, sg + pg * 256, 16 * 7168, sa);
        ST(6);
        await_l<C_R1, NCW>(L_SYNC);
        tile<16, W_YU, C_G>(mkrs(wu, 1024 * 3584 * 2), d0 + L_H2, vg, sg + pu * 256, 16 * 7168, sa);
        ST(8);
        VMWAIT(0);
        ST(9);
      } else {
        VMWAIT(0);
        arrive<C_V>(sa);
      }
    } else {
      const int kvb = kv0 + lw * 16 * 256;
      static_assert(WQX < 4, "");
      tile<WQX + 1, WQX + 1, C_Q>(rk, L + L_K + lw * 4096, vk, kvb, 1024, sa);
      tile<4, W_XK, -1, 0, 0, WQX + 1>(rk, L + L_K + lw * 4096, vk, kvb, 1024, sa);
      tile<4, W_XK, -1>(rv, L + L_K + 16384 + lw * 4096, vv, kvb, 1024, sa);
      ST(3);
      const __amdgpu_buffer_rsrc_t ro = mkrs(wo, 4096 * 1024 * 2);
      const int vo = (l >> 5) * 2048 + (((l & 31) ^ ((2 * lw + (l >> 5)) << 1)) * 16);
      const int so = ((ku >> 3) * 256 + (ku & 7) * 16 + 2 * lw) * 2048 + ocq * 512;
      tile<TQX, W_XO, C_K, 1>(ro, d0 + L_H2, vo, so, 0, sa);
      ST(4);
      arrive<C_TQ>(sa);
      BAR();
      tile<NO1, W_XO, C_K, 1, W_XO - 1, TQX>(ro, d0 + L_H2, vo, so, 0, sa);
      tile<16, W_XO2, -1, 1, 0, NO1>(ro, d0 + L_H2, vo, so, 0, sa);
      ST(6);
      const __amdgpu_buffer_rsrc_t rd = mkrs(wd, mlp ? 3584 * 1024 * 2 : 0);
      const int vd = (l ^ (4 * lw)) * 16, sd = (fg * 64 + 2 * lw) * 2048 + hf * 1024;
      await_l<C_R0, NCW>(L_SYNC);
      tile<ND0, W_XO2, -1, 2>(rd, d0, vd, sd, 0, sa);
      VMWAIT(ND0);
      arrive<C_G>(sa);
      ST(7);
      BAR();
      BAR();
      __builtin_amdgcn_s_sleep(DLY_W);
      tile<16, W_XD, -1, 2, 0, ND0>(rd, d0, vd, sd, 0, sa);
      ST(8);
      VMWAIT(0);
      ST(9);
    }
    STOUT();
    return;
  }

  FRESH(c);
  UNITS();
  const int w = wv - NLW;
  const int g = l >> 4, col = l & 15, b8 = l & 7, q4 = (l >> 2) & 3, p4 = l & 3;
  const int lst = (l & 1) * 64 + g * 16;
  const int l32 = (col >> 3) * 64 + g * 16;
  const int trow = 4 * g + q4, sw256 = (trow & 7) << 1;
  int sa = l == 0 ? L_SYNC : L_JUNK + 4 * l;
  LAS unsigned char *hw = L + L_ACT + (l >> 3) * 512 + (l & 3) * 128 + w * 16 + ((l >> 2) & 1) * 8;
  LAS unsigned char *bact = L + L_ACT + g * 128 + b8 * 16;
  LAS unsigned char *syn = L + L_SYNC;
  LAS unsigned char *a64 = L + (64 * g + q4) * 256 + (p4 & 1) * 8;
  unsigned one = 1u;
  Tag T;
  const __amdgpu_buffer_rsrc_t rs = mkrs(ws, WS_END);
#define TAGS(MASK)                                                                                                  \
  const unsigned E = __builtin_amdgcn_readfirstlane(Ev);                                                            \
  const unsigned Tw = ((E & 0x7f7f7f7fu) + 0x01010101u) ^ (E & 0x80808080u);           \
  T.t0 = Tw & (MASK), T.t1 = (Tw >> 8) & (MASK), T.t2 = (Tw >> 16) & (MASK), T.t3 = (Tw >> 24) & (MASK);

  const int ru = (xcd * 16 + jj) * 2 + (w & 1);
  const int hu = (xcd * 4 + kq) * 8 + w;
  unsigned Ev;
  {
    LAS unsigned char *aq0 = L + trow * 128 + (p4 & 1) * 8 + (((4 * (w & 1) + (p4 >> 1)) ^ (((trow >> 1) & 3) << 1)) * 16);
    LAS unsigned char *aq1 = L + trow * 128 + (p4 & 1) * 8 + (((4 * (w & 1) + 2 + (p4 >> 1)) ^ (((trow >> 1) & 3) << 1)) * 16);
    int o_qs = WS_QP + (((b8 * 32 + (cg64 >> 1)) * 4 + kq) * 4 + (cg64 & 1) * 2 + w) * 128 + l32;
    LAS unsigned char *xa_ = L + L_X + w * 2048 + l * 16, *xo_ = L + L_X + w * 2048 + kq * 512 + l * 8;
    T.m = 0xfu;
    asm volatile("" : "+v"(xa_), "+v"(xo_), "+v"(hw), "+v"(bact), "+v"(aq0), "+v"(aq1), "+v"(syn), "+v"(o_qs), "+v"(one), "+v"(T.m), "+v"(sa));
    await<C_X, NLW>(syn);
    ST(1);
    {
      const v4u xa = *(LAS v4u *)xa_, xb = *(LAS v4u *)(xa_ + 1024);
      const v2u xo = *(LAS v2u *)xo_;
      f32x2 v[8];
#pragma unroll
      for (int i = 0; i < 4; ++i) {
        v[i] = f32x2{blo(xa[i]), bhi(xa[i])};
        v[4 + i] = f32x2{blo(xb[i]), bhi(xb[i])};
      }
      const f32x2 s = ((v[0] + v[1]) + (v[2] + v[3])) + ((v[4] + v[5]) + (v[6] + v[7]));
      const float mean = sum64(s.x + s.y) * (1.f / 1024.f);
      f32x2 qq = {0.f, 0.f};
#pragma unroll
      for (int i = 0; i < 8; ++i) qq += (v[i] - mean) * (v[i] - mean);
      const float var = rbf(sum64(qq.x + qq.y) * (1.f / 1024.f));
      const float m = rbf(mean), ri = __builtin_amdgcn_rcpf(rbf(__builtin_amdgcn_sqrtf(rbf(var + 1e-5f))));
      const unsigned d0 = pkbf(blo(xo.x) - m, bhi(xo.x) - m), d1 = pkbf(blo(xo.y) - m, bhi(xo.y) - m);
      *(LAS v2u *)hw = v2u{pkbf(blo(d0) * ri, bhi(d0) * ri), pkbf(blo(d1) * ri, bhi(d1) * ri)};
    }
    arrive<C_XN>(sa, one);
    ST(2);
    if (w < 2) {
      await<C_XN, NCW>(syn);
      v8s a0[8], a1[8];
      v4u bf[8];
      rd_b<8>(bact, bf);
      await<C_Q, NLW>(syn);
      Ev = *(LAS unsigned *)(syn + (L_EP - L_SYNC));
#pragma unroll
      for (int ks = 0; ks < 8; ++ks) {
        a0[ks] = trfrag(aq0, ks * 4096, ks * 4096 + 2048);
        a1[ks] = trfrag(aq1, ks * 4096, ks * 4096 + 2048);
      }
      SCHED();
      const unsigned E0 = __builtin_amdgcn_readfirstlane(Ev);
      T.t0 = E0 + 1u, T.t1 = (E0 >> 8) + 1u, T.t2 = (E0 >> 16) + 1u, T.t3 = (E0 >> 24) + 1u;
      stws(rs, o_qs, tag4(mm2<8>(a0, a1, bf, col < 8), T));
    } else {
      await<C_Q, NLW, 2>(syn);
      Ev = *(LAS unsigned *)(syn + (L_EP - L_SYNC));
    }
  }
  ST(3);
  TAGS(0xffu);
  T.m = 0xffu;
  Tag T4;
  T4.t0 = T.t0 & 0xfu, T4.t1 = T.t1 & 0xfu, T4.t2 = T.t2 & 0xfu, T4.t3 = T.t3 & 0xfu;
  SCHED();
  const int rpl = isX ? 16 : 48, nt = isX ? (w < 4 ? 1 : 0) : (w < 4 ? 2 : 1);
  LAS unsigned char *a256 = L + L_K + rpl * 1024 + (rpl * g + q4) * 256 + (p4 & 1) * 8 + (((2 * w + (p4 >> 1)) ^ sw256) * 16);
  LAS unsigned char *kp[4];
#pragma unroll
  for (int i = 0; i < 4; ++i) kp[i] = L + L_K + (rpl * q4 + 4 * w + p4) * 256 + (((4 * i + g) ^ col) * 16);
  LAS unsigned char *qw = L + L_ACT2 + ((l >> 3) & 3) * 512 + ((l >> 1) & 3) * 128 + w * 16 + (l & 1) * 8 + (l >> 5) * 4;
  LAS unsigned char *mxw = L + (l < 8 ? L_MX + l * 32 + w * 4 : L_JUNK + 256 + 4 * l), *mxr = L + L_MX + b8 * 32;
  LAS unsigned char *pw = bact + (w >> 1) * 512 + (w & 1) * 8;
  int o_qg = WS_QP + (xcd * 32 + akv * 8 + w) * 2048 + l * 16;
  int o_as = WS_AP + ((((b8 >> 1) * 8 + w) * 64 + xcd * 8 + as) * 4 + akv) * 128 + lst + (col < 8 ? 0 : OOB);
  int o_ms = WS_ML + (akv * 64 + xcd * 8 + as) * 128 + l * 16 + (l < 8 ? 0 : OOB);
  float xr = blo(*(LAS u16 *)(L + L_X + (hu * 32 + 4 * b8 + g) * 2));
  asm volatile("" : "+v"(xr), "+v"(a256), "+v"(kp[0]), "+v"(kp[1]), "+v"(kp[2]), "+v"(kp[3]), "+v"(qw), "+v"(mxw), "+v"(mxr), "+v"(pw), "+v"(o_qg),
               "+v"(o_as), "+v"(o_ms), "+v"(T.m));
  {
    await<C_TQ, NLW>(syn);
    v4u ra[2];
    issue<2, 1024>(rs, o_qg, ra);
    BAR();
    v8s kf[4], kg[4], af[6];
    await<C_K, NLW>(syn);
#pragma unroll
    for (int i = 0; i < 4; ++i) kf[i] = *(LAS v8s *)kp[i], kg[i] = *(LAS v8s *)(kp[i] + 8192);
    if (isX) af[0] = trfrag(a256, 0, 1024), af[1] = trfrag(a256, 2048, 3072);
    arrive<C_R0>(sa, one);
    f32x4 a[2];
    settle<2, 1024, 0xfu>(rs, o_qg, ra, a, T4);
    ST(4);
    const f32x4 s = a[0] + a[1];
    *(LAS unsigned *)qw = pkbf(rs32(s.x, s.z), rs32(s.y, s.w));
    arrive<C_QL>(sa, one);
    await<C_QL, NCW>(syn);
    ST(5);
    v4u qf[4];
    rd_b<4>(bact + 4096, qf);
    const float NINF = -__builtin_inff();
    f32x4 s1 = {NINF, NINF, NINF, NINF}, s2 = s1;
    if (nt > 0) s1 = score(mm<4>(kf, qf));
    if (nt > 1) s2 = score(mm<4>(kg, qf));
    *(LAS float *)mxw = mx32(mx16(fmx(fmx3(fmx3(s1.x, s1.y, s1.z), s1.w, s2.x), fmx3(s2.y, s2.z, s2.w))));
    arrive<C_MX>(sa, one);
    await<C_MX, NCW>(syn);
    const f32x4 m0 = *(LAS f32x4 *)mxr, m1 = *(LAS f32x4 *)(mxr + 16);
    const float M = fmx3(fmx3(m0.x, m0.y, m0.z), fmx3(m0.w, m1.x, m1.y), fmx3(m1.z, m1.w, m1.w));
    float ls = prob(s1, M, pw);
    if (nt > 1) ls += prob(s2, M, pw + 2048);
    ls = rs16(ls, ls);
    *(LAS float *)(mxw + 256) = rs32(ls, ls);
    f32x4 acc;
    arrive<C_P>(sa, one);
    await<C_P, NCW>(syn);
    if (w == 7) {
      const f32x4 l2 = *(LAS f32x4 *)(mxr + 256) + *(LAS f32x4 *)(mxr + 272);
      stws(rs, o_ms, tag4(f32x4{M, (l2.x + l2.y) + (l2.z + l2.w), 0.f, 0.f}, T));
    }
    if (isX) {
      const v4u p0 = *(LAS v4u *)bact, p1 = *(LAS v4u *)(bact + 512);
      acc = mfma(af[1], p1, mfma(af[0], p0, f32x4{0.f, 0.f, 0.f, 0.f}));
    } else {
      v4u pf[6];
      rd_b<6>(bact, pf);
      await<C_V, NLW>(syn);
      ST(6);
      rd_a<6, 2048>(a256, af);
      arrive<C_R1>(sa, one);
      acc = mm<6>(af, pf);
    }
    stws(rs, o_as, tag4(acc, T));
  }
  ST(7);
#define H_UNIT()                                                                                                                          \
  {                                               \
    f32x4 a[4];                                                                                                                           \
    gather<4, 1024>(rs, o_og, a, T);                                                                                                      \
    RETRY(1);                                                                                                                             \
    const f32x4 s = (a[0] + a[1]) + (a[2] + a[3]);                                                                                        \
    float t = rs16(rs32(s.x, s.z), rs32(s.y, s.w));                                                                                       \
    t += dpp<XOR8>(t);                                                                                                                    \
    const unsigned hb = asu(rbf(xr + rbf(t)));                                                                                            \
    const auto e = __builtin_amdgcn_permlane16_swap(hb, hb, false, false);                      \
    const auto f0 = __builtin_amdgcn_permlane32_swap(e[0], e[0], false, false), f1 = __builtin_amdgcn_permlane32_swap(e[1], e[1], false, false); \
    stws(rs, o_hs, v4u{f0[0] | T.t0, f1[0] | T.t1, f0[1] | T.t2, f1[1] | T.t3});                                                          \
    ST(11);                                                                                                                               \
  }
#define ML_POLL()                                                                                   \
  for (unsigned spins = 0;; ++spins) {                                                              \
    asm volatile("" ::: "memory");                                                                  \
    const v4u u = ldws(rs, o_mg);                                                                   \
    const unsigned ux = u.x ^ T.t0, uy = u.y ^ T.t1;                                                \
    mv = asf(ux), lv = asf(uy);                                                                     \
    const unsigned d = ((ux | uy) & 0xffu) | (u.z ^ T.t2) | (u.w ^ T.t3);                           \
    if (__builtin_amdgcn_ballot_w64(d != 0u) == 0ull || spins >= SPIN_CAP) break;                   \
    __builtin_amdgcn_s_sleep(SLP_M);                                                                \
  }
  int o_mg = WS_ML + (((l >> 3) & 3) * 64 + w * 8 + 4 * (l >> 5) + p4) * 128 + (2 * (ku >> 3) + ((l >> 2) & 1)) * 16;
  int o_og = WS_OP + hu * 4096 + l * 16, o_hs = WS_HB + hu * 128 + l * 16 + (l < 8 ? 0 : OOB);
  if (!isX) {
    LAS unsigned char *at = a64 + (((2 * w + (p4 >> 1)) ^ sw256) * 16), *at2 = at + L_H2;
    if (mlp) {
      const int gl = ((((col < 8 ? pg : pu) * 2 + (w >> 2)) * 2 + (col >> 3)) * 4 + gq) * 4 + (w & 3);
      int o_hg = WS_HB + w * 4096 + gq * 1024 + l * 16;
      int o_gs = WS_GP + gl * 512 + b8 * 64 + g * 16;
      asm volatile("" : "+v"(o_hg), "+v"(o_gs));
      v8s a0[8], a1[8];
      await<C_G, NLW, 2>(syn);
      rd_a<8, 2048>(at, a0);
      BAR();
      ST(8);
      v4u rh[1];
      issue<1, 0>(rs, o_hg, rh);
      rd_a<8, 2048>(at2, a1);
      {
        v4u ones[8];
#pragma unroll
        for (int ks = 0; ks < 8; ++ks) ones[ks] = v4u{0x3f803f80u, 0x3f803f80u, 0x3f803f80u, 0x3f803f80u};
        stws(rs, WS_CS + gl * 128 + lst + (b8 < 2 ? 0 : OOB), tag4(mm2<8>(a0, a1, ones, col < 8), T));
      }
      f32x4 h[1];
      settle<1, 0>(rs, o_hg, rh, h, T);
      ST(11);
      *(LAS v2u *)hw = v2u{__builtin_amdgcn_perm(asu(h[0].y), asu(h[0].x), 0x07060302u), __builtin_amdgcn_perm(asu(h[0].w), asu(h[0].z), 0x07060302u)};
      BAR();
      ST(12);
      v4u bf[8];
      rd_b<8>(bact, bf);
      SCHED();
      stws(rs, o_gs, tag4(mm2<8>(a0, a1, bf, col < 8), T));
      ST(10);
    } else {
      asm volatile("" : "+v"(o_mg), "+v"(o_og), "+v"(o_hs));
      float mv, lv;
      ML_POLL();
      f32x4 ap[4];
      gather<4, 512>(rs, WS_AP + (ku * 64 + w * 8 + 4 * (l >> 5)) * 512 + (l & 31) * 16, ap, T);
      BAR();
      ST(10);
      __builtin_amdgcn_s_sleep(DLY_RY);
      H_UNIT();
    }
    STOUT();
    return;
  }

  const int drow = 2 * (g >> 1) + (trow & 1), da = (trow >> 1) & 3;
  const int gt = fg * 32 + g * 4 + 2 * (w & 1) + ((l >> 3) & 1);
  LAS unsigned char *ao = L + L_H2 + (32 * da + drow) * 512 + (p4 & 1) * 8, *ad = L + (16 * da + drow) * 1024 + (p4 & 1) * 8;
  LAS unsigned char *ow = L + L_ACT2 + ((l >> 3) & 3) * 512 + p4 * 128 + w * 16 + ((l >> 2) & 1) * 8 + (l >> 5) * 4;
  LAS unsigned char *dw = L + L_ACT2 + (w & 1) * 512 + p4 * 128 + (2 * (w >> 1) + ((l >> 2) & 1)) * 16 + ((l >> 3) & 1) * 8 + g * 2;
  int o_ag = WS_AP + (ku * 64 + w * 8 + 4 * (l >> 5)) * 512 + (l & 31) * 16;
  int o_os = WS_OP + ((b8 * 32 + 8 * ocq + w) * 32 + ku) * 128 + l32;
  int o_hg = WS_HB + w * 4096 + l * 16, o_eh = WS_HB + ru * 128 + b8 * 16;
  int o_gg = WS_GP + gt * 512 + (w >> 1) * 128 + b8 * 16, o_cg = mlp ? WS_CS + gt * 128 + p4 * 16 : OOB;
  int o_ds = WS_DP + ((b8 * 32 + 16 * hf + 2 * w) * 56 + fg) * 128 + l32;
  int o_dg = WS_DP + ru * 7168 + l * 16, o_out = (ru * 32 + 4 * b8 + g) * 2 + (col < 8 ? 0 : OOB);
  asm volatile("" : "+v"(ow), "+v"(dw), "+v"(o_mg), "+v"(o_ag), "+v"(o_os), "+v"(o_og), "+v"(o_hs), "+v"(o_hg), "+v"(o_gg), "+v"(o_cg), "+v"(o_ds),
               "+v"(o_dg), "+v"(o_out), "+v"(o_eh));
  {
    v8s a0[4], a1[4];
    await<C_G, NLW, 1>(syn);
    rd_a<4, 4096>(ao + (((4 * w + (p4 >> 1)) ^ sw256) * 16), a0);
    rd_a<4, 4096>(ao + (((4 * w + 2 + (p4 >> 1)) ^ sw256) * 16), a1);
    ST(8);
    float mv, lv;
    v4u ra[4];
    ML_POLL();
    __builtin_amdgcn_s_sleep(DLY_M);
    issue<4, 512>(rs, o_ag, ra);
    float mm_ = fmx(mv, dpp<XOR1>(mv));
    mm_ = mx32(fmx(mm_, dpp<XOR2>(mm_)));
    const float wt = __builtin_amdgcn_exp2f((mv - mm_) * LOG2E);
    float ll = wt * lv;
    ll += dpp<XOR1>(ll);
    ll += dpp<XOR2>(ll);
    ll = rs32(ll, ll);
    const float cf = wt * __builtin_amdgcn_rcpf(ll);
    const float c0 = dpp<0x00>(cf), c1 = dpp<0x55>(cf), c2 = dpp<0xAA>(cf), c3 = dpp<0xFF>(cf);
    f32x4 a[4];
    settle<4, 512>(rs, o_ag, ra, a, T);
    ST(9);
    const f32x4 o = (a[0] * c0 + a[1] * c1) + (a[2] * c2 + a[3] * c3);
    *(LAS unsigned *)ow = pkbf(rs32(o.x, o.z), rs32(o.y, o.w));
    BAR();
    v4u bf[4];
    rd_b<4>(bact + 4096, bf);
    SCHED();
    stws(rs, o_os, tag4(mm2<4>(a0, a1, bf, col < 8), T));
  }
  ST(10);
  BAR();
  const unsigned th = now() + D_H * TK;
  if (!mlp) {
    __builtin_amdgcn_s_sleep(DLY_R);
    H_UNIT();
  }
  BAR();
  v8s a0[2], a1[2], a2[2], a3[2];
  rd_a<2, 8192>(ad + (((8 * w + (p4 >> 1)) ^ sw256) * 16), a0);
  rd_a<2, 8192>(ad + (((8 * w + 2 + (p4 >> 1)) ^ sw256) * 16), a1);
  rd_a<2, 8192>(ad + (((8 * w + 4 + (p4 >> 1)) ^ sw256) * 16), a2);
  rd_a<2, 8192>(ad + (((8 * w + 6 + (p4 >> 1)) ^ sw256) * 16), a3);
  until(th);
  v4u rc[2], re[1];
  {
    f32x4 h[4];
    gather<4, 1024>(rs, o_hg, h, T);
    issue<2, 2048>(rs, o_cg, rc);
    issue<1, 0>(rs, o_eh, re);
    const f32x4 sv = (h[0] + h[1]) + (h[2] + h[3]);
    const float mean = sum64((sv.x + sv.y) + (sv.z + sv.w)) * (1.f / 1024.f);
    const f32x4 d0_ = h[0] - mean, d1 = h[1] - mean, d2 = h[2] - mean, d3 = h[3] - mean;
    const f32x4 qv = (d0_ * d0_ + d1 * d1) + (d2 * d2 + d3 * d3);
    const float var = rbf(sum64((qv.x + qv.y) + (qv.z + qv.w)) * (1.f / 1024.f));
    *(LAS f32x2 *)(L + L_MX + w * 8) = f32x2{rbf(mean), __builtin_amdgcn_rcpf(rbf(__builtin_amdgcn_sqrtf(rbf(var + 1e-5f))))};
  }
  BAR();
  const unsigned tpoll = now() + D_W * TK;
  ST(12);
  if (c == 0 && w == 2 && l == 0) __hip_atomic_store((GAS unsigned *)ws, Tw, __ATOMIC_RELAXED, __HIP_MEMORY_SCOPE_AGENT);
  f32x4 he[1];
  settle<1, 0>(rs, o_eh, re, he, T);
  float hv = (g & 2) ? ((g & 1) ? he[0].w : he[0].z) : ((g & 1) ? he[0].y : he[0].x);
  PIN(hv);
  if (mlp) {
    const f32x2 st = *(LAS f32x2 *)(L + L_MX + (2 * (w >> 1) + ((l >> 2) & 1)) * 8);
    f32x4 cs[2], a[2];
    settle<2, 2048>(rs, o_cg, rc, cs, T);
    float cg = rs16(rs32(cs[0].x, cs[0].z), rs32(cs[0].y, cs[0].w)) * st.x, cu = rs16(rs32(cs[1].x, cs[1].z), rs32(cs[1].y, cs[1].w)) * st.x;
    asm volatile("" : "+v"(cg), "+v"(cu));
    until(tpoll);
    gather<2, 8192>(rs, o_gg, a, T);
    RETRY(6);
    ST(13);
    const unsigned gu = pkbf((rs16(rs32(a[0].x, a[0].z), rs32(a[0].y, a[0].w)) - cg) * st.y, (rs16(rs32(a[1].x, a[1].z), rs32(a[1].y, a[1].w)) - cu) * st.y);
    const float sg = rbf(__builtin_amdgcn_rcpf(1.f + __builtin_amdgcn_exp2f(blo(gu) * -LOG2E)));
    *(LAS u16 *)dw = (u16)pkbf(sg * bhi(gu), 0.f);
    BAR();
    v4u bf[2];
    rd_b<2>(bact + 4096, bf);
    SCHED();
    const f32x4 r0 = mm2<2>(a0, a1, bf, col < 8), r1 = mm2<2>(a2, a3, bf, col < 8);
    stws(rs, o_ds, tag4(r0, T));
    stws(rs, o_ds + 56 * 128, tag4(r1, T));
    ST(14);
  } else {
    until(tpoll + 85 * TK);
  }
  if (w < 2) {
    __builtin_amdgcn_s_sleep(DLY_E);
    f32x4 a[7];
    gather<7, 1024>(rs, o_dg, a, T);
    RETRY(2);
    const f32x4 s = ((a[0] + a[1]) + (a[2] + a[3])) + ((a[4] + a[5]) + a[6]);
    float t = rs16(rs32(s.x, s.z), rs32(s.y, s.w));
    t += dpp<XOR8>(t);
    __builtin_amdgcn_raw_buffer_store_b16((u16)pkbf(hv + rbf(t), 0.f), mkrs(out, 8 * 1024 * 2), o_out, 0, 0);
  }
  STOUT();
}

extern "C" void kernel_launch(void *const *d_in, const int *in_sizes, int n_in, void *d_out, int out_size, void *d_ws, size_t ws_size,
    hipStream_t stream) {
  (void)in_sizes, (void)n_in, (void)out_size, (void)ws_size;
  (void)hipFuncSetAttribute((const void *)blk_kernel, hipFuncAttributeMaxDynamicSharedMemorySize, L_END);
  blk_kernel<<<256, NTHR, L_END, stream>>>((const u16 *)d_in[0], (const u16 *)d_in[3], (const u16 *)d_in[1], (const u16 *)d_in[2],
      (const u16 *)d_in[4], (const u16 *)d_in[6], (const u16 *)d_in[7], (const u16 *)d_in[8], (u16 *)d_out, (unsigned char *)d_ws);
}
```

```cpp
#include <hip/hip_runtime.h>

#define GAS __attribute__((address_space(1)))
#define LAS __attribute__((address_space(3)))
typedef unsigned short u16;
typedef unsigned long long u64;
typedef unsigned v4u __attribute__((ext_vector_type(4)));
typedef unsigned v2u __attribute__((ext_vector_type(2)));
typedef float f32x4 __attribute__((ext_vector_type(4)));
typedef short v4s __attribute__((ext_vector_type(4)));
typedef short v8s __attribute__((ext_vector_type(8)));
typedef __bf16 v8bf __attribute__((ext_vector_type(8)));
typedef __bf16 v2bf __attribute__((ext_vector_type(2)));
typedef float f32x2 __attribute__((ext_vector_type(2)));

namespace {
constexpr int AUXW = 2;
constexpr int RX = 32, RY = 64 - RX;
constexpr int WQX = 5, W_XK = 6, W_XO = 5, NO1 = 5, W_XO2 = 10, W_XD = 10, W_YK = 6, W_YG = 2, NG1 = 6, W_YU = 8, ATAIL = 4;
constexpr int NQ0 = 8, ND0 = 0, DLY_W = 4, SLP_M = 4, SLP_P = 2;
constexpr int D_Q = 250, D_A = 310, D_R = 60, D_H = 165, D_W = 28, D_E = 65;
constexpr int NCW = 8, NLW = 4, NTHR = (NCW + NLW) * 64;
constexpr int WS_QP = 4096;
constexpr int WS_P1 = WS_QP + 4 * 8 * 4096 * 4;
constexpr int WS_AP = WS_P1 + 128;
constexpr int WS_ML = WS_AP + 32 * 256 * 128;
constexpr int WS_P2 = WS_ML + 256 * 128;
constexpr int WS_OP = WS_P2 + 128;
constexpr int WS_HB = WS_OP + 32 * 8 * 1024 * 4;
constexpr int WS_P3 = WS_HB + 8 * 1024 * 4;
constexpr int WS_GP = WS_P3 + 128;
constexpr int WS_CS = WS_GP + 56 * 16384;
constexpr int WS_P4 = WS_CS + 56 * 4096;
constexpr int WS_DP = WS_P4 + 128;
constexpr int WS_P5 = WS_DP + 56 * 8 * 1024 * 4;
constexpr int WS_END = WS_P5 + 128;
constexpr int OOB = 0x40000000;
constexpr int L_K = 32768, L_H2 = 65536, L_X = 131072, L_ACT = L_X + 16384, L_ACT2 = L_ACT + 4096, L_MX = L_ACT2 + 2048, L_LS = L_MX + 256,
              L_SYNC = L_LS + 256, L_EP = L_SYNC + 64, L_JUNK = L_EP + 256, L_END = L_JUNK + 2048;
enum { C_X = 0, C_XN, C_Q1, C_Q, C_K, C_TQ, C_QL, C_MX, C_P, C_V, C_G, C_R0, C_R1, C_TA, C_N };
constexpr int SC1 = 16;
constexpr unsigned SPIN_CAP = 1u << 16;
constexpr float LOG2E = 1.4426950408889634f;

#define BAR() asm volatile("s_waitcnt lgkmcnt(0)\n\ts_barrier" ::: "memory")
#define VMWAIT(n) asm volatile("s_waitcnt vmcnt(%0)" ::"n"(n) : "memory")
#define SCHED() __builtin_amdgcn_sched_barrier(0)
#define PIN(x) asm volatile("" : "+v"(x))

__device__ __forceinline__ float asf(unsigned u) { return __builtin_bit_cast(float, u); }
__device__ __forceinline__ unsigned asu(float f) { return __builtin_bit_cast(unsigned, f); }
__device__ __forceinline__ unsigned pkbf(float lo, float hi) {
  f32x2 v = {lo, hi};
  return __builtin_bit_cast(unsigned, __builtin_convertvector(v, v2bf));
}
__device__ __forceinline__ float blo(unsigned p) { return asf(p << 16); }
__device__ __forceinline__ float bhi(unsigned p) { return asf(p & 0xffff0000u); }
__device__ __forceinline__ float rbf(float f) { return blo(pkbf(f, f)); }
__device__ __forceinline__ float fmx(float a, float b) { return __builtin_fmaxf(a, b); }
template <int CTRL> __device__ __forceinline__ float dpp(float x) {
  return asf(__builtin_amdgcn_mov_dpp(asu(x), CTRL, 0xf, 0xf, true));
}
constexpr int XOR1 = 0xB1, XOR2 = 0x4E, XOR7 = 0x141, XOR8 = 0x128;
__device__ __forceinline__ float rs32(float a, float b) {
  auto r = __builtin_amdgcn_permlane32_swap(asu(a), asu(b), false, false);
  return asf(r[0]) + asf(r[1]);
}
__device__ __forceinline__ float rs16(float a, float b) {
  auto r = __builtin_amdgcn_permlane16_swap(asu(a), asu(b), false, false);
  return asf(r[0]) + asf(r[1]);
}
__device__ __forceinline__ float mx32(float a) {
  auto r = __builtin_amdgcn_permlane32_swap(asu(a), asu(a), false, false);
  return fmx(asf(r[0]), asf(r[1]));
}
__device__ __forceinline__ float mx16(float a) {
  auto r = __builtin_amdgcn_permlane16_swap(asu(a), asu(a), false, false);
  return fmx(asf(r[0]), asf(r[1]));
}
__device__ __forceinline__ float sum64(float x) {
  x += dpp<XOR1>(x);
  x += dpp<XOR2>(x);
  x += dpp<XOR7>(x);
  x += dpp<XOR8>(x);
  x = rs16(x, x);
  return rs32(x, x);
}
__device__ __forceinline__ float fmx3(float a, float b, float c) {
  float r;
  asm("v_max3_f32 %0, %1, %2, %3" : "=v"(r) : "v"(a), "v"(b), "v"(c));
  return r;
}
__device__ __forceinline__ f32x4 score(f32x4 acc) {
  const float SCL = 0.08838834764831845f;
  const unsigned r0 = pkbf(acc.x, acc.y), r1 = pkbf(acc.z, acc.w);
  const unsigned s0 = pkbf(blo(r0) * SCL, bhi(r0) * SCL), s1 = pkbf(blo(r1) * SCL, bhi(r1) * SCL);
  return f32x4{blo(s0), bhi(s0), blo(s1), bhi(s1)};
}
__device__ __forceinline__ float prob(f32x4 s, float M, LAS unsigned char *pw) {
  const float p0 = __builtin_amdgcn_exp2f((s.x - M) * LOG2E), p1 = __builtin_amdgcn_exp2f((s.y - M) * LOG2E),
              p2 = __builtin_amdgcn_exp2f((s.z - M) * LOG2E), p3 = __builtin_amdgcn_exp2f((s.w - M) * LOG2E);
  *(LAS v2u *)pw = v2u{pkbf(p0, p1), pkbf(p2, p3)};
  return (p0 + p1) + (p2 + p3);
}
struct Tag {
  unsigned t0, t1, t2, t3;
  unsigned m;
};
__device__ __forceinline__ v4u tag4(f32x4 v, const Tag &T) {
  v4u o;
  o.x = (T.m & T.t0) | (~T.m & asu(v.x));
  o.y = (T.m & T.t1) | (~T.m & asu(v.y));
  o.z = (T.m & T.t2) | (~T.m & asu(v.z));
  o.w = (T.m & T.t3) | (~T.m & asu(v.w));
  return o;
}
__device__ __forceinline__ v4u ldws(__amdgpu_buffer_rsrc_t rs, int off) { return __builtin_amdgcn_raw_buffer_load_b128(rs, off, 0, SC1); }
__device__ __forceinline__ void stws(__amdgpu_buffer_rsrc_t rs, int off, v4u v) { __builtin_amdgcn_raw_buffer_store_b128(v, rs, off, 0, SC1); }
__device__ __forceinline__ f32x4 mfma(v8s a, v4u b, f32x4 c) {
  return __builtin_amdgcn_mfma_f32_16x16x32_bf16(__builtin_bit_cast(v8bf, a), __builtin_bit_cast(v8bf, b), c, 0, 0, 0);
}
__device__ __forceinline__ v8s trfrag(LAS unsigned char *p, int off0, int off1) {
  const v4s a = __builtin_amdgcn_ds_read_tr16_b64_v4i16((LAS v4s *)(p + off0));
  const v4s b = __builtin_amdgcn_ds_read_tr16_b64_v4i16((LAS v4s *)(p + off1));
  return __builtin_shufflevector(a, b, 0, 1, 2, 3, 4, 5, 6, 7);
}
template <int NK, int KS> __device__ __forceinline__ void rd_a(LAS unsigned char *a, v8s (&af)[NK]) {
#pragma unroll
  for (int ks = 0; ks < NK; ++ks) af[ks] = trfrag(a, ks * KS, ks * KS + KS / 2);
}
template <int NK> __device__ __forceinline__ void rd_b(LAS unsigned char *b, v4u (&bf)[NK]) {
#pragma unroll
  for (int ks = 0; ks < NK; ++ks) bf[ks] = *(LAS v4u *)(b + ks * 512);
}
template <int NK> __device__ __forceinline__ f32x4 mm(const v8s (&af)[NK], const v4u (&bf)[NK]) {
  SCHED();
  f32x4 acc = {0.f, 0.f, 0.f, 0.f};
#pragma unroll
  for (int ks = 0; ks < NK; ++ks) acc = mfma(af[ks], bf[ks], acc);
  return acc;
}
template <int NK> __device__ __forceinline__ f32x4 mm2(const v8s (&a0)[NK], const v8s (&a1)[NK], const v4u (&bf)[NK], bool first) {
  f32x4 c0 = {0.f, 0.f, 0.f, 0.f}, c1 = {0.f, 0.f, 0.f, 0.f};
#pragma unroll
  for (int ks = 0; ks < NK; ++ks) {
    c0 = mfma(a0[ks], bf[ks], c0);
    c1 = mfma(a1[ks], bf[ks], c1);
  }
  return first ? c0 : c1;
}
template <int NK> __device__ __forceinline__ void pin_a(v8s (&af)[NK]) {
#pragma unroll
  for (int ks = 0; ks < NK; ++ks) PIN(af[ks]);
}
template <int N, int STEP> __device__ __forceinline__ void issue(__amdgpu_buffer_rsrc_t rs, int off, v4u (&a)[N]) {
  asm volatile("" ::: "memory");
#pragma unroll
  for (int i = 0; i < N; ++i) a[i] = ldws(rs, off + i * STEP);
}
template <int N, unsigned MASK> __device__ __forceinline__ bool stale(const v4u (&a)[N], f32x4 (&v)[N], const Tag &T) {
  unsigned d = 0;
#pragma unroll
  for (int i = 0; i < N; ++i) {
    const v4u u = {a[i].x ^ T.t0, a[i].y ^ T.t1, a[i].z ^ T.t2, a[i].w ^ T.t3};
    d |= (u.x | u.y) | (u.z | u.w);
    v[i] = __builtin_bit_cast(f32x4, u);
  }
  return __builtin_amdgcn_ballot_w64((d & MASK) != 0u) != 0ull;
}
template <int N, int STEP, unsigned MASK = 0xffu>
__device__ __forceinline__ void settle(__amdgpu_buffer_rsrc_t rs, int off, const v4u (&a)[N], f32x4 (&v)[N], const Tag &T) {
  if (__builtin_expect(stale<N, MASK>(a, v, T), 0)) {
    unsigned spins = 0;
    bool again;
#pragma unroll 1
    do {
      v4u r[N];
      __builtin_amdgcn_s_sleep(SLP_P);
      issue<N, STEP>(rs, off, r);
      again = stale<N, MASK>(r, v, T);
    } while (again && ++spins < SPIN_CAP);
  }
}
template <int N, int STEP, unsigned MASK = 0xffu>
__device__ __forceinline__ void gather(__amdgpu_buffer_rsrc_t rs, int off, f32x4 (&v)[N], const Tag &T) {
  v4u a[N];
  issue<N, STEP>(rs, off, a);
  settle<N, STEP, MASK>(rs, off, a, v, T);
}
__device__ __forceinline__ unsigned now() { return (unsigned)__builtin_amdgcn_s_memrealtime(); }
__device__ __forceinline__ void until(unsigned t) {
  for (;;) {
    const int rem = (int)(t - now());
    if (rem <= 0) break;
    if (rem > 12) __builtin_amdgcn_s_sleep(2);
  }
}
__device__ __forceinline__ __amdgpu_buffer_rsrc_t mkrs(const void *p, int bytes) {
  return __builtin_amdgcn_make_buffer_rsrc((void *)p, 0, bytes, 0x00020000);
}
template <int AUX, int IMM = 0> __device__ __forceinline__ void dma1(__amdgpu_buffer_rsrc_t rs, LAS unsigned char *dst, int voff, int soff) {
  __builtin_amdgcn_raw_ptr_buffer_load_lds(rs, (LAS void *)dst, 16, voff, soff, IMM, AUX);
}
template <int K> __device__ __forceinline__ void arrive(int sa, unsigned one = 1u) {
  asm volatile("ds_add_u32 %0, %1 offset:%2" ::"v"(sa), "v"(one), "n"(4 * K) : "memory");
}
template <int K, int N, int SLEEP = 0, typename P> __device__ __forceinline__ void await(P syn) {
  unsigned v, left, t;
  asm volatile("s_mov_b32 %1, 0x1000000\n"
               ".Lspin%=:\n\t"
               "ds_read_b32 %0, %3 offset:%4\n\t"
               ".if %6\n\ts_sleep %6\n\t.endif\n\t"
               "s_sub_u32 %1, %1, 1\n\t"
               "s_cbranch_scc1 .Ldone%=\n\t"
               "s_waitcnt lgkmcnt(0)\n\t"
               "v_readfirstlane_b32 %2, %0\n\t"
               "s_cmp_lt_u32 %2, %5\n\t"
               "s_cbranch_scc1 .Lspin%=\n"
               ".Ldone%=:\n\t"
               "s_waitcnt lgkmcnt(0)"
               : "=&v"(v), "=&s"(left), "=&s"(t)
               : "v"(syn), "n"(4 * K), "n"(N), "n"(SLEEP)
               : "memory", "scc");
}
#define await_l await
template <int NP, int W, int LANDED, int MODE = 0, int LI = W - 1, int LO = 0>
__device__ __forceinline__ void tile(__amdgpu_buffer_rsrc_t rs, LAS unsigned char *dst, int voff, int soff, int sstep, int sa) {
#pragma unroll 1
  for (int i = LO; i < NP; ++i) {
    if (W < 64) VMWAIT(W - 1);
    if (LANDED >= 0 && i == LI) {
      VMWAIT(LI < 63 ? LI : 63);
      arrive<(LANDED < 0 ? 0 : LANDED)>(sa);
    }
    if (MODE == 0) dma1<AUXW>(rs, dst + i * 1024, voff, soff + i * sstep);
    if (MODE == 2) dma1<AUXW>(rs, dst + i * 1024, voff ^ ((i & 1) * 32), soff + ((i >> 1) * 8 + (i & 1)) * 2048);
    if (MODE == 1) dma1<AUXW>(rs, dst + i * 1024, voff, soff + (((i >> 1) & 1) * 128 + (i >> 2) * 1024 + (i & 1) * 8) * 2048);
  }
}
}

#define FRESH(v) asm volatile("" : "+s"(v))

__global__ void __launch_bounds__(NTHR, 3)
    blk_kernel(const u16 *x, const u16 *wq, const u16 *kc, const u16 *vc, const u16 *wo, const u16 *wg, const u16 *wu, const u16 *wd, u16 *out,
        unsigned char *ws) {
  extern __shared__ __attribute__((aligned(16))) unsigned char smem_[];
  LAS unsigned char *L = (LAS unsigned char *)smem_;
  const int tid = threadIdx.x, l = tid & 63, wv = __builtin_amdgcn_readfirstlane(tid >> 6);
  int c = blockIdx.x;
  const bool isX = (c & 8) == 0;
#define XCD (c & 7)
#define JJ (c >> 4)
#define UNITS()                                                                                                         \
  const int xcd = XCD, j = c >> 3, jj = JJ;                                                                             \
  const int kq = j & 3, cg64 = (j >> 2) * 8 + ((xcd + (j >> 2)) & 7);            \
  const int akv = j >> 3, as = j & 7;                                     \
  const int ku = 4 * xcd + (jj >> 2), ocq = jj & 3;                              \
  const bool mlp = jj < 14;                                                                                             \
  const int fg = (jj >> 1) * 8 + xcd, hf = jj & 1;                       \
  const int yu = jj * 8 + xcd, gq = yu / 28, pg = yu - 28 * gq, pu = pg < 26 ? pg + 2 : pg - 26;    \
  (void)kq, (void)cg64, (void)akv, (void)as, (void)ku, (void)ocq, (void)mlp, (void)fg, (void)hf, (void)gq, (void)pu;

  const int lw = wv & 3;
  asm volatile("ds_write_b32 %0, %1" ::"v"(L_SYNC + 4 * (l & 15)), "v"(0u) : "memory");
  int vx = l * 16, vq = (l >> 3) * 8192 + ((l & 7) ^ (((l >> 4) & 3) << 1)) * 16;
  int sq = (((c >> 3) & 3) * 256 + lw * 64) * 8192 + ((c >> 5) * 8 + ((XCD + (c >> 5)) & 7)) * 128, sx = lw * 4096;
  LAS unsigned char *dq = L + lw * 8192 + ((~c & 8) << 13), *dx = L + L_X + lw * 4096;
  asm volatile("" : "+v"(vx), "+v"(vq), "+s"(sq), "+s"(sx), "+s"(dq), "+s"(dx));
  asm volatile("s_waitcnt lgkmcnt(0)\n\ts_barrier" ::"s"(x), "s"(wq), "s"(kc), "s"(vc), "s"(wo), "s"(wg), "s"(wu), "s"(wd), "s"(out), "s"(ws)
               : "memory");
  if (__builtin_expect(wv < NLW, 1)) {
    __builtin_amdgcn_s_waitcnt(0x0F70);
    {
      const __amdgpu_buffer_rsrc_t rx = mkrs(x, 8 * 1024 * 2);
      dma1<0, 0>(rx, dx, vx, sx);
      dma1<0, 1024>(rx, dx, vx, sx);
      dma1<0, 2048>(rx, dx, vx, sx);
      dma1<0, 3072>(rx, dx, vx, sx);
    }
    tile<NQ0, 64, -1>(mkrs(wq, 1024 * 4096 * 2), dq, vq, sq, 8 * 8192, 0);
    const int sa = l == 0 ? L_SYNC : L_JUNK + 4 * l;
    FRESH(c);
    UNITS();
    const int kv0 = ((xcd * 4 + akv) * 4096 + 3072 + (as >> 1) * 256) * 256;
    tile<8, 64, -1, 0, 0, NQ0>(mkrs(wq, 1024 * 4096 * 2), dq, vq, sq, 8 * 8192, 0);
    VMWAIT(8);
    arrive<C_X>(sa);
    __builtin_amdgcn_raw_ptr_buffer_load_lds(mkrs(ws, 4096), (LAS void *)(L + L_EP), 4, 0, 0, 0, SC1);
    const __amdgpu_buffer_rsrc_t rk = mkrs(kc, 8 * 4 * 4096 * 128 * 2), rv = mkrs(vc, 8 * 4 * 4096 * 128 * 2);
    const int r4 = l >> 4;
    const int v256 = ((l & 15) ^ ((4 * (lw & 1) + r4) << 1)) * 16, vv = r4 * 256 + v256, vk = r4 * 256 + (((l & 15) ^ (4 * lw + r4)) * 16);
    LAS unsigned char *d0 = L + lw * 16384;
    if (!isX) {
      const int kvb = kv0 + (4 * RX + lw * RY) * 256;
      tile<WQX + 1, WQX + 1, C_Q>(rk, L + L_K + lw * RY * 256, vk, kvb, 1024, sa);
      tile<RY / 4, W_YK, -1, 0, 0, WQX + 1>(rk, L + L_K + lw * RY * 256, vk, kvb, 1024, sa);
      tile<RY / 4, W_YK, C_K>(rv, L + L_K + RY * 1024 + lw * RY * 256, vv, kvb, 1024, sa);
      if (mlp) {
        const int sg = (gq * 256 + lw * 4) * 7168, vg = r4 * 7168 + v256;
        await_l<C_R0, NCW>(L_SYNC);
        tile<NG1, W_YG, C_V>(mkrs(wg, 1024 * 3584 * 2), d0, vg, sg + pg * 256, 16 * 7168, sa);
        tile<16, W_YU, -1, 0, 0, NG1>(mkrs(wg, 1024 * 3584 * 2), d0, vg, sg + pg * 256, 16 * 7168, sa);
        await_l<C_R1, NCW>(L_SYNC);
        tile<16, W_YU, C_G>(mkrs(wu, 1024 * 3584 * 2), d0 + L_H2, vg, sg + pu * 256, 16 * 7168, sa);
        VMWAIT(0);
      } else {
        VMWAIT(0);
        arrive<C_V>(sa);
      }
    } else {
      const int kvb = kv0 + lw * RX * 256;
      static_assert(WQX < RX / 4 && W_XK <= RX / 4 && W_YK <= RY / 4 && W_YG <= NG1 && W_XO <= NO1, "");
      tile<WQX + 1, WQX + 1, C_Q>(rk, L + lw * RX * 256, vk, kvb, 1024, sa);
      tile<RX / 4, W_XK, -1, 0, 0, WQX + 1>(rk, L + lw * RX * 256, vk, kvb, 1024, sa);
      tile<RX / 4, W_XK, C_K>(rv, L + RX * 1024 + lw * RX * 256, vv, kvb, 1024, sa);
      const __amdgpu_buffer_rsrc_t ro = mkrs(wo, 4096 * 1024 * 2);
      const int vo = (l >> 5) * 2048 + (((l & 31) ^ ((2 * lw + (l >> 5)) << 1)) * 16);
      const int so = ((ku >> 3) * 256 + (ku & 7) * 16 + 2 * lw) * 2048 + ocq * 512;
      await<C_TQ, 2>(L_SYNC);
      tile<NO1, W_XO, C_V, 1>(ro, d0 + L_H2, vo, so, 0, sa);
      tile<16, W_XO2, -1, 1, 0, NO1>(ro, d0 + L_H2, vo, so, 0, sa);
      const __amdgpu_buffer_rsrc_t rd = mkrs(wd, mlp ? 3584 * 1024 * 2 : 0);
      const int vd = (l ^ (4 * lw)) * 16, sd = (fg * 64 + 2 * lw) * 2048 + hf * 1024;
      if (ND0 > 0) await_l<C_R1, NCW>(L_SYNC);
      tile<ND0, W_XO2, -1, 2>(rd, d0, vd, sd, 0, sa);
      VMWAIT(ND0 + ATAIL);
      arrive<C_TA>(sa);
      VMWAIT(ND0);
      arrive<C_G>(sa);
      BAR();
      BAR();
      __builtin_amdgcn_s_sleep(DLY_W);
      tile<16, W_XD, -1, 2, 0, ND0>(rd, d0, vd, sd, 0, sa);
      VMWAIT(0);
    }
    return;
  }

  FRESH(c);
  UNITS();
  const int w = wv - NLW;
  const int g = l >> 4, col = l & 15, b8 = l & 7, q4 = (l >> 2) & 3, p4 = l & 3;
  const int lst = (l & 1) * 64 + g * 16;
  const int l32 = (col >> 3) * 64 + g * 16;
  const int trow = 4 * g + q4, sw256 = (trow & 7) << 1;
  int sa = l == 0 ? L_SYNC : L_JUNK + 4 * l;
  LAS unsigned char *hw = L + L_ACT + (l >> 3) * 512 + (l & 3) * 128 + w * 16 + ((l >> 2) & 1) * 8;
  LAS unsigned char *bact = L + L_ACT + g * 128 + b8 * 16;
  LAS unsigned char *syn = L + L_SYNC;
  LAS unsigned char *a64 = L + (64 * g + q4) * 256 + (p4 & 1) * 8;
  unsigned one = 1u;
  Tag T;
  const __amdgpu_buffer_rsrc_t rs = mkrs(ws, WS_END);
#define TAGS(MASK)                                                                                                  \
  const unsigned E = __builtin_amdgcn_readfirstlane(Ev);                                                            \
  const unsigned Tw = ((E & 0x7f7f7f7fu) + 0x01010101u) ^ (E & 0x80808080u);           \
  T.t0 = Tw & (MASK), T.t1 = (Tw >> 8) & (MASK), T.t2 = (Tw >> 16) & (MASK), T.t3 = (Tw >> 24) & (MASK);

  const int ru = (xcd * 16 + jj) * 2 + (w & 1);
  const int hu = (xcd * 4 + kq) * 8 + w;
  unsigned Ev, tq;
  {
    LAS unsigned char *aq0 = L + (isX ? L_H2 : 0) + trow * 128 + (p4 & 1) * 8 + (((4 * (w & 1) + (p4 >> 1)) ^ (((trow >> 1) & 3) << 1)) * 16);
    LAS unsigned char *aq1 = L + (isX ? L_H2 : 0) + trow * 128 + (p4 & 1) * 8 + (((4 * (w & 1) + 2 + (p4 >> 1)) ^ (((trow >> 1) & 3) << 1)) * 16);
    int o_qs = WS_QP + (((b8 * 32 + (cg64 >> 1)) * 4 + kq) * 4 + (cg64 & 1) * 2 + w) * 128 + l32;
    LAS unsigned char *xa_ = L + L_X + w * 2048 + l * 16, *xo_ = L + L_X + w * 2048 + kq * 512 + l * 8;
    T.m = 0x3u;
    asm volatile("" : "+v"(xa_), "+v"(xo_), "+v"(hw), "+v"(bact), "+v"(aq0), "+v"(aq1), "+v"(syn), "+v"(o_qs), "+v"(one), "+v"(T.m), "+v"(sa));
    await<C_X, NLW>(syn);
    tq = now() + D_Q;
    {
      const v4u xa = *(LAS v4u *)xa_, xb = *(LAS v4u *)(xa_ + 1024);
      const v2u xo = *(LAS v2u *)xo_;
      f32x2 v[8];
#pragma unroll
      for (int i = 0; i < 4; ++i) {
        v[i] = f32x2{blo(xa[i]), bhi(xa[i])};
        v[4 + i] = f32x2{blo(xb[i]), bhi(xb[i])};
      }
      const f32x2 s = ((v[0] + v[1]) + (v[2] + v[3])) + ((v[4] + v[5]) + (v[6] + v[7]));
      const float mean = sum64(s.x + s.y) * (1.f / 1024.f);
      f32x2 qq = {0.f, 0.f};
#pragma unroll
      for (int i = 0; i < 8; ++i) qq += (v[i] - mean) * (v[i] - mean);
      const float var = rbf(sum64(qq.x + qq.y) * (1.f / 1024.f));
      const float m = rbf(mean), ri = __builtin_amdgcn_rcpf(rbf(__builtin_amdgcn_sqrtf(rbf(var + 1e-5f))));
      const unsigned d0 = pkbf(blo(xo.x) - m, bhi(xo.x) - m), d1 = pkbf(blo(xo.y) - m, bhi(xo.y) - m);
      *(LAS v2u *)hw = v2u{pkbf(blo(d0) * ri, bhi(d0) * ri), pkbf(blo(d1) * ri, bhi(d1) * ri)};
    }
    arrive<C_XN>(sa, one);
    if (w < 2) {
      await<C_XN, NCW>(syn);
      v8s a0[8], a1[8];
      v4u bf[8];
      rd_b<8>(bact, bf);
      await<C_Q, NLW>(syn);
      Ev = *(LAS unsigned *)(syn + (L_EP - L_SYNC));
#pragma unroll
      for (int ks = 0; ks < 8; ++ks) {
        a0[ks] = trfrag(aq0, ks * 4096, ks * 4096 + 2048);
        a1[ks] = trfrag(aq1, ks * 4096, ks * 4096 + 2048);
      }
      SCHED();
      const unsigned E0 = __builtin_amdgcn_readfirstlane(Ev);
      T.t0 = E0 + 1u, T.t1 = (E0 >> 8) + 1u, T.t2 = (E0 >> 16) + 1u, T.t3 = (E0 >> 24) + 1u;
      const v4u qv = __builtin_bit_cast(v4u, mm2<8>(a0, a1, bf, col < 8)) + 2u;
      stws(rs, o_qs, tag4(__builtin_bit_cast(f32x4, qv), T));
      arrive<C_TQ>(sa, one);
    } else {
      await<C_Q, NLW, 2>(syn);
      Ev = *(LAS unsigned *)(syn + (L_EP - L_SYNC));
    }
  }
  TAGS(0xffu);
  T.m = 0xffu;
  Tag T4;
  T4.t0 = T.t0 & 3u, T4.t1 = T.t1 & 3u, T4.t2 = T.t2 & 3u, T4.t3 = T.t3 & 3u;
  SCHED();
  const int rpl = isX ? RX : RY, nt = (4 * w < rpl) + (4 * w + 32 < rpl), kb = isX ? 0 : L_K;
  LAS unsigned char *a256 = L + kb + rpl * 1024 + (rpl * g + q4) * 256 + (p4 & 1) * 8 + (((2 * w + (p4 >> 1)) ^ sw256) * 16);
  LAS unsigned char *kp[4];
#pragma unroll
  for (int i = 0; i < 4; ++i) kp[i] = L + kb + (rpl * q4 + 4 * w + p4) * 256 + (((4 * i + g) ^ col) * 16);
  LAS unsigned char *qw = L + L_ACT2 + ((l >> 3) & 3) * 512 + ((l >> 1) & 3) * 128 + w * 16 + (l & 1) * 8 + (l >> 5) * 4;
  LAS unsigned char *mxw = L + (l < 8 ? L_MX + l * 32 + w * 4 : L_JUNK + 256 + 4 * l), *mxr = L + L_MX + b8 * 32;
  LAS unsigned char *pw = bact + (w >> 1) * 512 + (w & 1) * 8;
  int o_qg = WS_QP + (xcd * 32 + akv * 8 + w) * 2048 + l * 16;
  int o_as = WS_AP + ((((b8 >> 1) * 8 + w) * 64 + xcd * 8 + as) * 4 + akv) * 128 + lst + (col < 8 ? 0 : OOB);
  int o_ms = WS_ML + (akv * 64 + xcd * 8 + as) * 128 + l * 16 + (l < 8 ? 0 : OOB);
  float xr = blo(*(LAS u16 *)(L + L_X + (hu * 32 + 4 * b8 + g) * 2));
  asm volatile("" : "+v"(xr), "+v"(a256), "+v"(kp[0]), "+v"(kp[1]), "+v"(kp[2]), "+v"(kp[3]), "+v"(qw), "+v"(mxw), "+v"(mxr), "+v"(pw), "+v"(o_qg),
               "+v"(o_as), "+v"(o_ms), "+v"(T.m));
  {
    until(tq);
    v4u ra[2];
    issue<2, 1024>(rs, o_qg, ra);
    v8s kf[4], kg[4];
    await<C_K, NLW>(syn);
#pragma unroll
    for (int i = 0; i < 4; ++i) {
      kf[i] = *(LAS v8s *)kp[i];
      if (RY > 32) kg[i] = *(LAS v8s *)(kp[i] + 8192);
    }
    arrive<C_R0>(sa, one);
    f32x4 a[2];
    settle<2, 1024, 3u>(rs, o_qg, ra, a, T4);
    const f32x4 s = a[0] + a[1];
    *(LAS unsigned *)qw = pkbf(rs32(s.x, s.z), rs32(s.y, s.w));
    arrive<C_QL>(sa, one);
    await<C_QL, NCW>(syn);
    v4u qf[4];
    rd_b<4>(bact + 4096, qf);
    const float NINF = -__builtin_inff();
    f32x4 s1 = {NINF, NINF, NINF, NINF}, s2 = s1;
    if (RX == 32 || nt > 0) s1 = score(mm<4>(kf, qf));
    if (RY > 32 && nt > 1) s2 = score(mm<4>(kg, qf));
    *(LAS float *)mxw = mx32(mx16(RY > 32 ? fmx(fmx3(fmx3(s1.x, s1.y, s1.z), s1.w, s2.x), fmx3(s2.y, s2.z, s2.w)) : fmx(fmx3(s1.x, s1.y, s1.z), s1.w)));
    arrive<C_MX>(sa, one);
    await<C_MX, NCW>(syn);
    const f32x4 m0 = *(LAS f32x4 *)mxr, m1 = *(LAS f32x4 *)(mxr + 16);
    const float M = fmx3(fmx3(m0.x, m0.y, m0.z), fmx3(m0.w, m1.x, m1.y), fmx3(m1.z, m1.w, m1.w));
    float ls = prob(s1, M, pw);
    if (RY > 32 && nt > 1) ls += prob(s2, M, pw + 2048);
    ls = rs16(ls, ls);
    *(LAS float *)(mxw + 256) = rs32(ls, ls);
    f32x4 acc;
    arrive<C_P>(sa, one);
    await<C_P, NCW>(syn);
    if (w == 7) {
      const f32x4 l2 = *(LAS f32x4 *)(mxr + 256) + *(LAS f32x4 *)(mxr + 272);
      stws(rs, o_ms, tag4(f32x4{M, (l2.x + l2.y) + (l2.z + l2.w), 0.f, 0.f}, T));
    }
#define PV(NK)                    \
  {                               \
    v4u pf[NK];                   \
    v8s af[NK];                   \
    rd_b<NK>(bact, pf);           \
    await<C_V, NLW>(syn);         \
    rd_a<NK, 2048>(a256, af);     \
    arrive<C_R1>(sa, one);        \
    acc = mm<NK>(af, pf);         \
  }
    if (RX != RY && isX) PV(RX / 8) else PV(RY / 8)
    stws(rs, o_as, tag4(acc, T));
  }
#define H_UNIT()                                                                                                                          \
  {                                               \
    f32x4 a[4];                                                                                                                           \
    gather<4, 1024>(rs, o_og, a, T);                                                                                                      \
    const f32x4 s = (a[0] + a[1]) + (a[2] + a[3]);                                                                                        \
    float t = rs16(rs32(s.x, s.z), rs32(s.y, s.w));                                                                                       \
    t += dpp<XOR8>(t);                                                                                                                    \
    const unsigned hb = asu(rbf(xr + rbf(t)));                                                                                            \
    const auto e = __builtin_amdgcn_permlane16_swap(hb, hb, false, false);                      \
    const auto f0 = __builtin_amdgcn_permlane32_swap(e[0], e[0], false, false), f1 = __builtin_amdgcn_permlane32_swap(e[1], e[1], false, false); \
    stws(rs, o_hs, v4u{f0[0] | T.t0, f1[0] | T.t1, f0[1] | T.t2, f1[1] | T.t3});                                                          \
  }
#define ML_POLL(FIRST, AGAIN)                                                                       \
  for (unsigned spins = 0;; ++spins) {                                                              \
    asm volatile("" ::: "memory");                                                                  \
    const v4u u = ldws(rs, o_mg);                                                                   \
    if (spins == 0) { FIRST }                                                                       \
    const unsigned ux = u.x ^ T.t0, uy = u.y ^ T.t1;                                                \
    mv = asf(ux), lv = asf(uy);                                                                     \
    const unsigned d = ((ux | uy) & 0xffu) | (u.z ^ T.t2) | (u.w ^ T.t3);                           \
    if (__builtin_amdgcn_ballot_w64(d != 0u) == 0ull || spins >= SPIN_CAP) {                        \
      if (spins != 0) { AGAIN }                                                                     \
      break;                                                                                        \
    }                                                                                               \
    __builtin_amdgcn_s_sleep(SLP_M);                                                                \
  }
#define AP_ISSUE() issue<4, 512>(rs, o_ag, ra)
  int o_ag = WS_AP + (ku * 64 + w * 8 + 4 * (l >> 5)) * 512 + (l & 31) * 16;
  int o_mg = WS_ML + (((l >> 3) & 3) * 64 + w * 8 + 4 * (l >> 5) + p4) * 128 + (2 * (ku >> 3) + ((l >> 2) & 1)) * 16;
  int o_og = WS_OP + hu * 4096 + l * 16, o_hs = WS_HB + hu * 128 + l * 16 + (l < 8 ? 0 : OOB);
  if (!isX) {
    LAS unsigned char *at = a64 + (((2 * w + (p4 >> 1)) ^ sw256) * 16), *at2 = at + L_H2;
    if (mlp) {
      const int gl = ((((col < 8 ? pg : pu) * 2 + (w >> 2)) * 2 + (col >> 3)) * 4 + gq) * 4 + (w & 3);
      int o_hg = WS_HB + w * 4096 + gq * 1024 + l * 16;
      int o_gs = WS_GP + gl * 512 + b8 * 64 + g * 16;
      asm volatile("" : "+v"(o_hg), "+v"(o_gs));
      v8s a0[8], a1[8];
      await<C_G, NLW, 2>(syn);
      rd_a<8, 2048>(at, a0);
      BAR();
      v4u rh[1];
      issue<1, 0>(rs, o_hg, rh);
      rd_a<8, 2048>(at2, a1);
      {
        v4u ones[8];
#pragma unroll
        for (int ks = 0; ks < 8; ++ks) ones[ks] = v4u{0x3f803f80u, 0x3f803f80u, 0x3f803f80u, 0x3f803f80u};
        stws(rs, WS_CS + gl * 128 + lst + (b8 < 2 ? 0 : OOB), tag4(mm2<8>(a0, a1, ones, col < 8), T));
      }
      f32x4 h[1];
      settle<1, 0>(rs, o_hg, rh, h, T);
      *(LAS v2u *)hw = v2u{__builtin_amdgcn_perm(asu(h[0].y), asu(h[0].x), 0x07060302u), __builtin_amdgcn_perm(asu(h[0].w), asu(h[0].z), 0x07060302u)};
      BAR();
      v4u bf[8];
      rd_b<8>(bact, bf);
      SCHED();
      stws(rs, o_gs, tag4(mm2<8>(a0, a1, bf, col < 8), T));
    } else {
      asm volatile("" : "+v"(o_mg), "+v"(o_og), "+v"(o_hs));
      float mv, lv;
      v4u ra[4];
      until(tq + D_A);
      ML_POLL(AP_ISSUE();, AP_ISSUE(););
      f32x4 ap[4];
      settle<4, 512>(rs, o_ag, ra, ap, T);
      BAR();
      until(now() + D_R + 22);
      H_UNIT();
    }
    return;
  }

  const int drow = 2 * (g >> 1) + (trow & 1), da = (trow >> 1) & 3;
  const int gt = fg * 32 + g * 4 + 2 * (w & 1) + ((l >> 3) & 1);
  LAS unsigned char *ao = L + L_H2 + (32 * da + drow) * 512 + (p4 & 1) * 8, *ad = L + (16 * da + drow) * 1024 + (p4 & 1) * 8;
  LAS unsigned char *ow = L + L_ACT2 + ((l >> 3) & 3) * 512 + p4 * 128 + w * 16 + ((l >> 2) & 1) * 8 + (l >> 5) * 4;
  LAS unsigned char *dw = L + L_ACT2 + (w & 1) * 512 + p4 * 128 + (2 * (w >> 1) + ((l >> 2) & 1)) * 16 + ((l >> 3) & 1) * 8 + g * 2;
  int o_os = WS_OP + ((b8 * 32 + 8 * ocq + w) * 32 + ku) * 128 + l32;
  int o_hg = WS_HB + w * 4096 + l * 16, o_eh = WS_HB + ru * 128 + b8 * 16;
  int o_gg = WS_GP + gt * 512 + (w >> 1) * 128 + b8 * 16, o_cg = mlp ? WS_CS + gt * 128 + p4 * 16 : OOB;
  int o_ds = WS_DP + ((b8 * 32 + 16 * hf + 2 * w) * 56 + fg) * 128 + l32;
  int o_dg = WS_DP + ru * 7168 + l * 16, o_out = (ru * 32 + 4 * b8 + g) * 2 + (col < 8 ? 0 : OOB);
  asm volatile("" : "+v"(ow), "+v"(dw), "+v"(o_mg), "+v"(o_ag), "+v"(o_os), "+v"(o_og), "+v"(o_hs), "+v"(o_hg), "+v"(o_gg), "+v"(o_cg), "+v"(o_ds),
               "+v"(o_dg), "+v"(o_out), "+v"(o_eh));
  {
    v8s a0[4], a1[4];
    LAS unsigned char *ao0 = ao + (((4 * w + (p4 >> 1)) ^ sw256) * 16), *ao1 = ao + (((4 * w + 2 + (p4 >> 1)) ^ sw256) * 16);
    asm volatile("" : "+v"(ao0), "+v"(ao1));
    await<C_TA, NLW, 1>(syn);
    until(tq + D_A);
    float mv, lv;
    v4u ra[4];
#define WO_READ() (await<C_G, NLW>(syn), rd_a<4, 4096>(ao0, a0), rd_a<4, 4096>(ao1, a1))
    ML_POLL(AP_ISSUE(); WO_READ();, AP_ISSUE(););
    float mm_ = fmx(mv, dpp<XOR1>(mv));
    mm_ = mx32(fmx(mm_, dpp<XOR2>(mm_)));
    const float wt = __builtin_amdgcn_exp2f((mv - mm_) * LOG2E);
    float ll = wt * lv;
    ll += dpp<XOR1>(ll);
    ll += dpp<XOR2>(ll);
    ll = rs32(ll, ll);
    const float cf = wt * __builtin_amdgcn_rcpf(ll);
    const float c0 = dpp<0x00>(cf), c1 = dpp<0x55>(cf), c2 = dpp<0xAA>(cf), c3 = dpp<0xFF>(cf);
    f32x4 a[4];
    settle<4, 512>(rs, o_ag, ra, a, T);
    const f32x4 o = (a[0] * c0 + a[1] * c1) + (a[2] * c2 + a[3] * c3);
    *(LAS unsigned *)ow = pkbf(rs32(o.x, o.z), rs32(o.y, o.w));
    BAR();
    v4u bf[4];
    rd_b<4>(bact + 4096, bf);
    SCHED();
    stws(rs, o_os, tag4(mm2<4>(a0, a1, bf, col < 8), T));
  }
  BAR();
  const unsigned th = now() + D_H;
  if (!mlp) {
    until(th - D_H + D_R);
    H_UNIT();
  }
  BAR();
  v8s a0[2], a1[2], a2[2], a3[2];
  rd_a<2, 8192>(ad + (((8 * w + (p4 >> 1)) ^ sw256) * 16), a0);
  rd_a<2, 8192>(ad + (((8 * w + 2 + (p4 >> 1)) ^ sw256) * 16), a1);
  rd_a<2, 8192>(ad + (((8 * w + 4 + (p4 >> 1)) ^ sw256) * 16), a2);
  rd_a<2, 8192>(ad + (((8 * w + 6 + (p4 >> 1)) ^ sw256) * 16), a3);
  until(th);
  v4u rc[2], re[1];
  {
    f32x4 h[4];
    gather<4, 1024>(rs, o_hg, h, T);
    issue<2, 2048>(rs, o_cg, rc);
    issue<1, 0>(rs, o_eh, re);
    const f32x4 sv = (h[0] + h[1]) + (h[2] + h[3]);
    const float mean = sum64((sv.x + sv.y) + (sv.z + sv.w)) * (1.f / 1024.f);
    const f32x4 d0_ = h[0] - mean, d1 = h[1] - mean, d2 = h[2] - mean, d3 = h[3] - mean;
    const f32x4 qv = (d0_ * d0_ + d1 * d1) + (d2 * d2 + d3 * d3);
    const float var = rbf(sum64((qv.x + qv.y) + (qv.z + qv.w)) * (1.f / 1024.f));
    *(LAS f32x2 *)(L + L_MX + w * 8) = f32x2{rbf(mean), __builtin_amdgcn_rcpf(rbf(__builtin_amdgcn_sqrtf(rbf(var + 1e-5f))))};
  }
  BAR();
  const unsigned tpoll = now() + D_W;
  if (c == 0 && w == 2 && l == 0) __hip_atomic_store((GAS unsigned *)ws, Tw, __ATOMIC_RELAXED, __HIP_MEMORY_SCOPE_AGENT);
  f32x4 he[1];
  settle<1, 0>(rs, o_eh, re, he, T);
  float hv = (g & 2) ? ((g & 1) ? he[0].w : he[0].z) : ((g & 1) ? he[0].y : he[0].x);
  PIN(hv);
  if (mlp) {
    const f32x2 st = *(LAS f32x2 *)(L + L_MX + (2 * (w >> 1) + ((l >> 2) & 1)) * 8);
    f32x4 cs[2], a[2];
    settle<2, 2048>(rs, o_cg, rc, cs, T);
    float cg = rs16(rs32(cs[0].x, cs[0].z), rs32(cs[0].y, cs[0].w)) * st.x, cu = rs16(rs32(cs[1].x, cs[1].z), rs32(cs[1].y, cs[1].w)) * st.x;
    asm volatile("" : "+v"(cg), "+v"(cu));
    until(tpoll);
    gather<2, 8192>(rs, o_gg, a, T);
    const unsigned gu = pkbf((rs16(rs32(a[0].x, a[0].z), rs32(a[0].y, a[0].w)) - cg) * st.y, (rs16(rs32(a[1].x, a[1].z), rs32(a[1].y, a[1].w)) - cu) * st.y);
    const float sg = rbf(__builtin_amdgcn_rcpf(1.f + __builtin_amdgcn_exp2f(blo(gu) * -LOG2E)));
    *(LAS u16 *)dw = (u16)pkbf(sg * bhi(gu), 0.f);
    BAR();
    v4u bf[2];
    rd_b<2>(bact + 4096, bf);
    SCHED();
    const f32x4 r0 = mm2<2>(a0, a1, bf, col < 8), r1 = mm2<2>(a2, a3, bf, col < 8);
    stws(rs, o_ds, tag4(r0, T));
    stws(rs, o_ds + 56 * 128, tag4(r1, T));
  } else {
    until(tpoll + 80);
  }
  if (w < 2) {
    until(now() + D_E);
    f32x4 a[7];
    gather<7, 1024>(rs, o_dg, a, T);
    const f32x4 s = ((a[0] + a[1]) + (a[2] + a[3])) + ((a[4] + a[5]) + a[6]);
    float t = rs16(rs32(s.x, s.z), rs32(s.y, s.w));
    t += dpp<XOR8>(t);
    __builtin_amdgcn_raw_buffer_store_b16((u16)pkbf(hv + rbf(t), 0.f), mkrs(out, 8 * 1024 * 2), o_out, 0, 0);
  }
}

extern "C" void kernel_launch(void *const *d_in, const int *in_sizes, int n_in, void *d_out, int out_size, void *d_ws, size_t ws_size,
    hipStream_t stream) {
  (void)in_sizes, (void)n_in, (void)out_size, (void)ws_size;
  (void)hipFuncSetAttribute((const void *)blk_kernel, hipFuncAttributeMaxDynamicSharedMemorySize, L_END);
  blk_kernel<<<256, NTHR, L_END, stream>>>((const u16 *)d_in[0], (const u16 *)d_in[3], (const u16 *)d_in[1], (const u16 *)d_in[2],
      (const u16 *)d_in[4], (const u16 *)d_in[6], (const u16 *)d_in[7], (const u16 *)d_in[8], (u16 *)d_out, (unsigned char *)d_ws);
}
```
